```python
import torch

def _softmax(x, axis=-1):
    return torch.softmax(x, dim=axis)
def _gelu(x):
    return torch.nn.functional.gelu(x, approximate="tanh")
def _silu(x):
    return torch.nn.functional.silu(x)
def _sigmoid(x):
    return torch.sigmoid(x)
def _layernorm(x, axis=-1, eps=1e-5):
    m = x.mean(dim=axis, keepdim=True)
    v = x.var(dim=axis, keepdim=True, unbiased=False)
    return (x - m) / torch.sqrt(v + eps)
def _scatter_add(tgt, idx, src, axis=0):
    return tgt.clone().index_add_(axis, idx, src)
def _segment_sum(src, seg, n):
    out = torch.zeros((n,) + src.shape[1:], dtype=src.dtype)
    return out.index_add_(0, seg, src)
def _pad(x, pw):
    flat = tuple(v for pair in reversed(pw) for v in pair)
    return torch.nn.functional.pad(x, flat)

kernel_name = 'standalone__gqa_block_cached__near_ridge'

def setup_inputs():

    _w = (lambda a: (lambda t: t.to(torch.bfloat16) if t.is_floating_point() else t)(torch.as_tensor(a)))
    B, Sq, D = 16, 80, 448
    Hq, Hkv, Sk, Dh = 16, 8, 512, 256
    assert Hq % Hkv == 0, (Hq, Hkv)
    return {
        "x": _w((lambda *s: torch.randn(*s, dtype=torch.bfloat16) * 0.8)(B, Sq, D)),
        "k_cache": _w((lambda *s: torch.randn(*s, dtype=torch.bfloat16) * 0.8)(B, Hkv, Sk, Dh)),
        "v_cache": _w((lambda *s: torch.randn(*s, dtype=torch.bfloat16) * 0.05)(B, Hkv, Sk, Dh)),
        "wq": _w((lambda *s: torch.randn(*s, dtype=torch.bfloat16) * 0.8)(D, Hq * Dh) * (D ** -0.5)),
        "wo": _w((lambda *s: torch.randn(*s, dtype=torch.bfloat16) * 0.05)(Hq * Dh, D) * ((Hq * Dh) ** -0.5)),
    }

def reference(x, k_cache, v_cache, wq, wo):
    B, Sq, D = x.shape
    Hq, Dh = 16, 256
    Hkv = k_cache.shape[1]
    rep = Hq // Hkv
    q = (lambda x, *d: x.permute(*d))((x @ wq).reshape(B, Sq, Hq, Dh), 0, 2, 1, 3)
    k = (lambda x, n, axis: torch.repeat_interleave(x, n, dim=axis))(k_cache, rep, axis=1)
    v = (lambda x, n, axis: torch.repeat_interleave(x, n, dim=axis))(v_cache, rep, axis=1)
    scores = torch.einsum("bhqd,bhkd->bhqk", q, k) * (Dh ** -0.5)
    p = _softmax(scores, axis=-1)
    o = torch.einsum("bhqk,bhkd->bhqd", p, v)
    return (lambda x, *d: x.permute(*d))(o, 0, 2, 1, 3).reshape(B, Sq, Hq * Dh) @ wo
```

```cpp
#include <hip/hip_runtime.h>
#include <hip/hip_bf16.h>
typedef unsigned short u16;
__device__ __forceinline__ float bf2f(u16 v){ return __uint_as_float(((unsigned)v)<<16); }
__device__ __forceinline__ u16 f2bf(float f){ __hip_bfloat16 b = __float2bfloat16(f); return *(u16*)&b; }
constexpr int B=16,SQ=80,D=448,HQ=16,HKV=8,SK=512,DH=256;
__global__ void n_qproj(const u16* x,const u16* wq,u16* q){
  int n = blockIdx.x*256+threadIdx.x; int m = blockIdx.y;
  float a=0; for(int k=0;k<D;k++) a += bf2f(x[m*D+k])*bf2f(wq[(size_t)k*4096+n]);
  q[(size_t)m*4096+n]=f2bf(a);
}
__global__ void n_attn(const u16* q,const u16* kc,const u16* vc,u16* o){
  int row=blockIdx.x, h=blockIdx.y, b=blockIdx.z; int t=threadIdx.x;
  __shared__ float qs[256]; __shared__ float s[512]; __shared__ float red[256];
  qs[t]=bf2f(q[((size_t)(b*SQ+row))*4096+h*256+t]); __syncthreads();
  const u16* K = kc + ((size_t)(b*HKV+h/2))*SK*DH; const u16* V = vc + ((size_t)(b*HKV+h/2))*SK*DH;
  for(int j=t;j<SK;j+=256){ float a=0; for(int d=0;d<DH;d++) a+=qs[d]*bf2f(K[(size_t)j*DH+d]); s[j]=a*0.0625f; }
  __syncthreads();
  float m=fmaxf(s[t],s[t+256]); red[t]=m; __syncthreads();
  for(int st=128;st>0;st>>=1){ if(t<st) red[t]=fmaxf(red[t],red[t+st]); __syncthreads(); }
  m=red[0]; __syncthreads();
  float e0=__expf(s[t]-m), e1=__expf(s[t+256]-m); s[t]=e0; s[t+256]=e1; red[t]=e0+e1; __syncthreads();
  for(int st=128;st>0;st>>=1){ if(t<st) red[t]+=red[t+st]; __syncthreads(); }
  float l=red[0];
  float a=0; for(int j=0;j<SK;j++) a+=s[j]*bf2f(V[(size_t)j*DH+t]);
  o[((size_t)(b*SQ+row))*4096+h*256+t]=f2bf(a/l);
}
__global__ void n_oproj(const u16* o,const u16* wo,u16* out){
  int n = threadIdx.x; int m = blockIdx.x;
  float a=0; for(int k=0;k<4096;k++) a += bf2f(o[(size_t)m*4096+k])*bf2f(wo[(size_t)k*D+n]);
  out[(size_t)m*D+n]=f2bf(a);
}
extern "C" void kernel_launch(void* const* d_in, const int* in_sizes, int n_in, void* d_out, int out_size, void* d_ws, size_t ws_size, hipStream_t stream){
  const u16* x=(const u16*)d_in[0]; const u16* kc=(const u16*)d_in[1]; const u16* vc=(const u16*)d_in[2];
  const u16* wq=(const u16*)d_in[3]; const u16* wo=(const u16*)d_in[4];
  u16* q=(u16*)d_ws; u16* o=q+(size_t)1280*4096;
  n_qproj<<<dim3(16,1280),256,0,stream>>>(x,wq,q);
  n_attn<<<dim3(SQ,HQ,B),256,0,stream>>>(q,kc,vc,o);
  n_oproj<<<1280,448,0,stream>>>(o,wo,(u16*)d_out);
}
```

```cpp
#include <hip/hip_runtime.h>
#include <utility>
typedef unsigned short u16; typedef unsigned u32; typedef unsigned long long u64;
typedef __bf16 bf16x8 __attribute__((ext_vector_type(8)));
typedef __bf16 bf16x4 __attribute__((ext_vector_type(4)));
typedef __bf16 bf16x2 __attribute__((ext_vector_type(2)));
typedef float f32x4 __attribute__((ext_vector_type(4)));
typedef float f32x2 __attribute__((ext_vector_type(2)));
typedef u32 u32x4 __attribute__((ext_vector_type(4)));
typedef u32 u32x2 __attribute__((ext_vector_type(2)));
#define LAS __attribute__((address_space(3)))
#define GAS __attribute__((address_space(1)))
#define DI __device__ __forceinline__
#define SB() __builtin_amdgcn_sched_barrier(0)
template <int... I> using iseq = std::integer_sequence<int, I...>;
template <int N> using mkseq = std::make_integer_sequence<int, N>;

#define ROTK 4
#define ROTV 0

constexpr int NB = 16, SQ = 80, DM = 448, HQ = 16, HKV = 8, SK = 512, DH = 256;
constexpr int LDS_BYTES = 163840, RING = 81920, RINGW = 20480;
constexpr int PART_BYTES = SQ * DM * 2;
constexpr size_t WS_FLAG = 0;
constexpr size_t WS_EPOCH = 8192;
constexpr size_t WS_PART = 16384;
constexpr size_t WS_STAMP = WS_PART + (size_t)NB * HQ * PART_BYTES;

enum { TW = 0, TK = 1, TV = 2, TO = 3 };
struct Sched {
  int type[54], idx[54], ninst[54], lds[54], rd[54], iss[54], use[54];
  int cum[55];
};
constexpr Sched make_sched() {
  Sched s{};
  const char* ord = "WWWKWWKWWKWKWWKWKWWKWK";
  int nw = 0, nk = 0, i = 0;
  for (; i < 22; ++i) { if (ord[i] == 'W') { s.type[i] = TW; s.idx[i] = nw++; } else { s.type[i] = TK; s.idx[i] = nk++; } }
  for (; i < 30; ++i) { s.type[i] = TK; s.idx[i] = nk++; }
  for (; i < 46; ++i) { s.type[i] = TV; s.idx[i] = i - 30; }
  for (; i < 54; ++i) { s.type[i] = TO; s.idx[i] = i - 46; }
  for (i = 0; i < 54; ++i) { s.ninst[i] = s.type[i] == TO ? 7 : 4; }
  s.cum[0] = 0; for (i = 0; i < 54; ++i) s.cum[i + 1] = s.cum[i] + s.ninst[i];
  for (i = 0; i < 54; ++i) s.use[i] = s.type[i] == TW ? s.idx[i] : s.type[i] == TK ? 14 + s.idx[i] : s.type[i] == TV ? 30 + s.idx[i] : 46 + s.idx[i];
  for (i = 21; i >= 0; --i) {
    if (s.type[i] == TW) s.rd[i] = s.idx[i] - 1;
    else s.rd[i] = (i == 21) ? 13 : s.rd[i + 1];
  }
  const int k8[8] = {15, 17, 19, 21, 22, 24, 26, 28};
  for (i = 22; i < 30; ++i) s.rd[i] = k8[i - 22];
  for (i = 30; i < 46; ++i) s.rd[i] = i - 1;
  for (i = 46; i < 54; ++i) s.rd[i] = i - 1;
  for (i = 0; i < 46; ++i) s.lds[i] = (i % 5) * 4096;
  for (i = 46; i < 54; ++i) { int k = (i - 46) % 3; s.lds[i] = k == 0 ? 4096 : k == 1 ? 12288 : -1; }
  for (i = 0; i < 5; ++i) s.iss[i] = -1;
  for (i = 5; i < 46; ++i) s.iss[i] = s.rd[i - 5] + 1;
  s.iss[46] = s.rd[42] + 1;
  s.iss[47] = s.rd[44] + 1;
  s.iss[48] = 46;
  for (i = 49; i < 54; ++i) s.iss[i] = s.rd[i - 3] + 1;
  return s;
}
constexpr Sched SC = make_sched();
constexpr int last_issued(int step) { int r = -1; for (int i = 0; i < 54; ++i) if (SC.iss[i] <= step) r = i; return r; }
constexpr int last_read(int step) { int r = -1; for (int i = 0; i < 54; ++i) if (SC.rd[i] <= step) r = i; return r; }
constexpr bool sched_ok() {
  for (int i = 1; i < 54; ++i) { if (SC.iss[i] < SC.iss[i - 1] || SC.rd[i] < SC.rd[i - 1]) return false; }
  for (int i = 0; i < 54; ++i) { if (SC.rd[i] < SC.iss[i] || SC.use[i] <= SC.rd[i]) return false; }
  return true;
}
static_assert(sched_ok(), "schedule");

struct Ctx {
  LAS char* lds;
  u32 ldsb, ringb;
  __amdgpu_buffer_rsrc_t rW, rK, rV, rO;
  u32 vW[4], vK[4], vV[4], vO[7];
  u32 aW[4], aK[4], aV[4], aO, aO2, aX[2], aB;
  u32 rotk, rotv, o2b;
  bf16x8 A[2][7];
  bf16x8 B[2][5];
  bf16x8 Kr[16][4];
  bf16x8 Qf[8][5];
  f32x4 acc[7][5];
  f32x4 S[8][5];
};

template <int IMM> DI void dma(u32 m0v, u32 voff, __amdgpu_buffer_rsrc_t rs, u32 soff) {
  asm volatile("s_mov_b32 m0, %0\n\ts_nop 0\n\tbuffer_load_dwordx4 %1, %2, %3 offen offset:%4 lds" :: "s"(m0v), "v"(voff), "s"(rs), "s"(soff), "i"(IMM) : "memory");
}
template <int N> DI void wait_vm() { asm volatile("s_waitcnt vmcnt(%0)" :: "i"(N) : "memory"); }
DI void wait_lgkm0() { asm volatile("s_waitcnt lgkmcnt(0)" ::: "memory"); }
DI void barrier() { asm volatile("s_waitcnt lgkmcnt(0)\n\ts_barrier" ::: "memory"); }
DI u32 cvtpk(float a, float b) { f32x2 v = {a, b}; return __builtin_bit_cast(u32, __builtin_convertvector(v, bf16x2)); }
DI bf16x8 ld128(Ctx& c, u32 off) { return *(LAS bf16x8*)(c.lds + off); }
DI bf16x8 ldtr(Ctx& c, u32 off0, u32 off1) {
  bf16x4 a = __builtin_amdgcn_ds_read_tr16_b64_v4bf16((LAS bf16x4*)(c.lds + off0));
  bf16x4 b = __builtin_amdgcn_ds_read_tr16_b64_v4bf16((LAS bf16x4*)(c.lds + off1));
  return __builtin_shufflevector(a, b, 0, 1, 2, 3, 4, 5, 6, 7);
}

template <int I, int J> DI void issue1(Ctx& c) {
  constexpr int ty = SC.type[I], ix = SC.idx[I];
  if constexpr (ty == TW) dma<J * 1024>(c.ringb + SC.lds[I], c.vW[J], c.rW, (u32)(ix * 32 * 8192));
  else if constexpr (ty == TK) dma<J * 1024>(c.ringb + SC.lds[I], c.vK[J], c.rK, (u32)((((ix >> 1) + c.rotk) & 7) * 8192 + (ix & 1) * 256));
  else if constexpr (ty == TV) dma<J * 1024>(c.ringb + SC.lds[I], c.vV[J], c.rV, (u32)(((ix + c.rotv) & 15) * 16384));
  else { const u32 base = SC.lds[I] < 0 ? c.o2b : c.ringb + SC.lds[I];
         dma<(J & 3) * 1024>(base + (J >> 2) * 4096, c.vO[J], c.rO, (u32)(ix * 32 * 896)); }
}
template <int G> DI void issue_g(Ctx& c) {
  constexpr int I = [] { int i = 0; while (SC.cum[i + 1] <= G) ++i; return i; }();
  issue1<I, G - SC.cum[I]>(c);
}
template <int G0, int... K> DI void issue_range(Ctx& c, iseq<K...>) { (issue_g<G0 + K>(c), ...); }

template <int I> DI void read_item(Ctx& c) {
  constexpr int ty = SC.type[I], ix = SC.idx[I], buf = SC.use[I] & 1;
  if constexpr (ty == TW) {
#pragma unroll
    for (int j = 0; j < 4; ++j) c.A[buf][j] = ldtr(c, c.aW[j] + SC.lds[I], c.aW[j] + SC.lds[I] + 512);
#pragma unroll
    for (int q = 0; q < 5; ++q) c.B[buf][q] = ld128(c, c.aX[ix & 1] + ((ix >> 1) * 10 + 2 * q) * 1024);
  } else if constexpr (ty == TK) {
#pragma unroll
    for (int s = 0; s < 4; ++s) c.Kr[ix][s] = ld128(c, c.aK[s] + SC.lds[I]);
  } else if constexpr (ty == TV) {
#pragma unroll
    for (int j = 0; j < 4; ++j) c.A[buf][j] = ldtr(c, c.aV[j] + SC.lds[I], c.aV[j] + SC.lds[I] + 2048);
    if constexpr (ix > 0) {
#pragma unroll
      for (int q = 0; q < 5; ++q) c.B[buf][q] = ld128(c, c.aB + (ix * 5 + q) * 1024);
    }
  } else {
    const u32 base = SC.lds[I] < 0 ? c.aO2 : c.aO + SC.lds[I];
#pragma unroll
    for (int j = 0; j < 7; ++j) c.A[buf][j] = ldtr(c, base + 32 * j, base + 32 * j + 3584);
    if constexpr (ix > 0) {
#pragma unroll
      for (int q = 0; q < 5; ++q) c.B[buf][q] = ld128(c, c.aB + (ix * 5 + q) * 1024);
    }
  }
}
template <int I0, int... K> DI void read_range(Ctx& c, iseq<K...>) { (read_item<I0 + K>(c), ...); }

#define MFMA(a, b, cc) __builtin_amdgcn_mfma_f32_16x16x32_bf16(a, b, cc, 0, 0, 0)
template <int S, int J> DI void mgroup(Ctx& c) {
  constexpr int buf = S & 1;
  if constexpr (S < 14 || S >= 30) {
    constexpr bool first = (S == 0 || S == 30 || S == 46);
#pragma unroll
    for (int q = 0; q < 5; ++q) c.acc[J][q] = MFMA(c.A[buf][J], c.B[buf][q], (first ? f32x4{0, 0, 0, 0} : c.acc[J][q]));
  } else {
    constexpr int u = S - 14, t = u >> 1, hf = u & 1;
#pragma unroll
    for (int q = 0; q < 5; ++q) c.S[t][q] = MFMA(c.Kr[u][J], c.Qf[hf * 4 + J][q], ((hf == 0 && J == 0) ? f32x4{0, 0, 0, 0} : c.S[t][q]));
  }
}
template <int S> DI void step(Ctx& c) {
  constexpr int i0 = last_issued(S - 1) + 1, i1 = last_issued(S);
  constexpr int g0 = SC.cum[i0], ng = SC.cum[i1 + 1] - g0, nh = (ng + 1) / 2;
  constexpr int r0 = last_read(S - 1) + 1, r1 = last_read(S);
  constexpr int NG = S >= 46 ? 7 : 4;
  wait_lgkm0(); SB();
  mgroup<S, 0>(c); SB();
  issue_range<g0>(c, mkseq<nh>{}); SB();
  mgroup<S, 1>(c); SB();
  issue_range<g0 + nh>(c, mkseq<ng - nh>{}); SB();
  if constexpr (NG == 7) { mgroup<S, 2>(c); SB(); }
  if constexpr (r1 >= r0) { wait_vm<SC.cum[i1 + 1] - SC.cum[r1 + 1]>(); read_range<r0>(c, mkseq<r1 - r0 + 1>{}); }
  if constexpr (NG == 4) { mgroup<S, 2>(c); mgroup<S, 3>(c); }
  else { mgroup<S, 3>(c); mgroup<S, 4>(c); mgroup<S, 5>(c); mgroup<S, 6>(c); }
  SB();
}
template <int S0, int... K> DI void steps(Ctx& c, iseq<K...>) { (step<S0 + K>(c), ...); }

#define NSTAMP 12
#define STAMP(n)

__global__ __launch_bounds__(256, 1) void gqa_fused(const u16* __restrict__ x, const u16* __restrict__ kc, const u16* __restrict__ vc,
                                                    const u16* __restrict__ wq, const u16* __restrict__ wo, u16* __restrict__ out, char* __restrict__ ws) {
  extern __shared__ __attribute__((aligned(16))) char smem[];
  Ctx c;
  c.lds = (LAS char*)smem;
  c.ldsb = (u32)(size_t)c.lds;
  const u32 tid = threadIdx.x, lane = tid & 63, wave = __builtin_amdgcn_readfirstlane(tid >> 6);
  const u32 bx = blockIdx.x, kvh = bx & 7, par = (bx >> 3) & 1, b = bx >> 4, h = kvh * 2 + par;
  const u32 q15 = lane & 15, g = lane >> 4, qp = (lane >> 2) & 3, p = lane & 3;
  STAMP(0);
  c.ringb = c.ldsb + RING + wave * RINGW;
  c.o2b = c.ldsb + 40960 + wave * 10240;
  c.rotk = par * ROTK; c.rotv = par * ROTV;
  const __amdgpu_buffer_rsrc_t rX = __builtin_amdgcn_make_buffer_rsrc((void*)(x + (size_t)b * SQ * DM), 0, SQ * DM * 2, 0x00020000);
  c.rW = __builtin_amdgcn_make_buffer_rsrc((void*)(wq + h * 256 + wave * 64), 0, (DM - 1) * 8192 + 128, 0x00020000);
  c.rK = __builtin_amdgcn_make_buffer_rsrc((void*)(kc + ((size_t)(b * HKV + kvh) * SK + wave * 128) * DH), 0, 128 * 512, 0x00020000);
  c.rV = __builtin_amdgcn_make_buffer_rsrc((void*)(vc + (size_t)(b * HKV + kvh) * SK * DH + wave * 64), 0, (SK - 1) * 512 + 128, 0x00020000);
  c.rO = __builtin_amdgcn_make_buffer_rsrc((void*)(wo + (size_t)h * 256 * DM + wave * 112), 0, 255 * 896 + 224, 0x00020000);
  const u32 r8 = lane >> 3, c8 = lane & 7;
  const u32 vX = r8 * 896 + ((c8 ^ ((4 * (wave & 1) + (lane >> 4)) & 7)) << 4);
#pragma unroll
  for (int i = 0; i < 4; ++i) {
    c.vW[i] = (8 * i + r8) * 8192 + ((c8 ^ ((r8 & 2) | ((i & 1) << 2))) << 4) - i * 1024;
    c.vK[i] = (4 * i + g) * 512 + ((q15 ^ (4 * i + g)) << 4) - i * 1024;
    c.vV[i] = (8 * i + r8) * 512 + ((c8 ^ (r8 & 6)) << 4) - i * 1024;
  }
  asm volatile("s_nop 4" ::: "memory");
#pragma unroll
  for (int j = 0; j < 18; ++j) {
    u32 pc = 4 * j + wave; if (j == 17) pc = pc >= 70 ? pc - 2 : pc;
    const u32 seg = pc / 10, rg = pc - seg * 10;
    dma<0>(c.ldsb + pc * 1024, vX, rX, rg * 7168 + seg * 128);
  }
  issue_range<0>(c, mkseq<20>{});
  const u32 ro = RING + wave * RINGW;
#pragma unroll
  for (int j = 0; j < 4; ++j) {
    c.aW[j] = ro + g * 1024 + qp * 128 + (((2 * j + (p >> 1)) ^ ((qp & 2) | ((g & 1) << 2))) << 4) + (p & 1) * 8;
    c.aK[j] = ro + q15 * 256 + (((4 * j + g) ^ q15) << 4);
    const u32 rw = 4 * (g & 1) + qp;
    c.aV[j] = ro + (g >> 1) * 1024 + rw * 128 + (((2 * j + (p >> 1)) ^ (rw & 6)) << 4) + (p & 1) * 8;
  }
#pragma unroll
  for (int i = 0; i < 2; ++i) c.aX[i] = (q15 >> 3) * 1024 + (q15 & 7) * 128 + (((4 * i + g) ^ ((q15 >> 1) & 7)) << 4);
  c.aB = lane * 16;
  c.aO = ro + (4 * g + qp) * 224 + p * 8;
  c.aO2 = 40960 + wave * 10240 + (4 * g + qp) * 224 + p * 8;
#pragma unroll
  for (int i = 0; i < 7; ++i) { const u32 jj = 64 * i + lane, rw = jj / 14, cc = jj - rw * 14; c.vO[i] = rw * 896 + cc * 16 - (i & 3) * 1024; }

  wait_vm<16>();
  barrier();
  STAMP(1);
  read_item<0>(c);
  steps<0>(c, mkseq<14>{});
  STAMP(2);
  barrier();
  {
    const u32 wb = q15 * 16 + (g >> 1) * 256 + (g & 1) * 8 + wave * 10240;
#pragma unroll
    for (int j = 0; j < 4; ++j)
#pragma unroll
      for (int q = 0; q < 5; ++q) {
        u32x2 v = {cvtpk(c.acc[j][q][0], c.acc[j][q][1]), cvtpk(c.acc[j][q][2], c.acc[j][q][3])};
        *(LAS u32x2*)(c.lds + wb + (j >> 1) * 5120 + q * 1024 + (j & 1) * 512) = v;
      }
  }
  barrier();
#pragma unroll
  for (int s = 0; s < 8; ++s)
#pragma unroll
    for (int q = 0; q < 5; ++q) c.Qf[s][q] = ld128(c, c.aB + (s * 5 + q) * 1024);
  STAMP(3);
  steps<14>(c, mkseq<16>{});
  STAMP(4);
  float lsum[5];
  {
    float m[5];
#pragma unroll
    for (int q = 0; q < 5; ++q) {
      float v = fmaxf(fmaxf(c.S[0][q][0], c.S[0][q][1]), fmaxf(c.S[0][q][2], c.S[0][q][3]));
#pragma unroll
      for (int t = 1; t < 8; ++t) v = fmaxf(fmaxf(fmaxf(v, c.S[t][q][0]), c.S[t][q][1]), fmaxf(c.S[t][q][2], c.S[t][q][3]));
      auto r1 = __builtin_amdgcn_permlane16_swap(__float_as_uint(v), __float_as_uint(v), false, false);
      v = fmaxf(__uint_as_float(r1[0]), __uint_as_float(r1[1]));
      auto r2 = __builtin_amdgcn_permlane32_swap(__float_as_uint(v), __float_as_uint(v), false, false);
      m[q] = fmaxf(__uint_as_float(r2[0]), __uint_as_float(r2[1]));
    }
    if (g == 0) {
#pragma unroll
      for (int q = 0; q < 5; ++q) *(LAS float*)(c.lds + 71680 + (q * 16 + q15) * 16 + wave * 4) = m[q];
    }
    barrier();
    constexpr float CS = 1.4426950408889634f / 16.0f;
#pragma unroll
    for (int q = 0; q < 5; ++q) {
      f32x4 mm = *(LAS f32x4*)(c.lds + 71680 + (q * 16 + q15) * 16);
      m[q] = -fmaxf(fmaxf(mm[0], mm[1]), fmaxf(mm[2], mm[3])) * CS;
    }
    u32x4 pf[4][5];
#pragma unroll
    for (int q = 0; q < 5; ++q) {
      float s0 = 0.f, s1 = 0.f;
#pragma unroll
      for (int t = 0; t < 8; ++t) {
        float e0 = __builtin_amdgcn_exp2f(fmaf(c.S[t][q][0], CS, m[q])), e1 = __builtin_amdgcn_exp2f(fmaf(c.S[t][q][1], CS, m[q]));
        float e2 = __builtin_amdgcn_exp2f(fmaf(c.S[t][q][2], CS, m[q])), e3 = __builtin_amdgcn_exp2f(fmaf(c.S[t][q][3], CS, m[q]));
        s0 += e0 + e2; s1 += e1 + e3;
        pf[t >> 1][q][(t & 1) * 2] = cvtpk(e0, e1); pf[t >> 1][q][(t & 1) * 2 + 1] = cvtpk(e2, e3);
      }
      float v = s0 + s1;
      auto r1 = __builtin_amdgcn_permlane16_swap(__float_as_uint(v), __float_as_uint(v), false, false);
      v = __uint_as_float(r1[0]) + __uint_as_float(r1[1]);
      auto r2 = __builtin_amdgcn_permlane32_swap(__float_as_uint(v), __float_as_uint(v), false, false);
      lsum[q] = __uint_as_float(r2[0]) + __uint_as_float(r2[1]);
    }
    barrier();
#pragma unroll
    for (int u = 0; u < 4; ++u) {
      const u32 ks = wave * 4 + ((u + (c.rotk >> 1)) & 3);
      const u32 slot = (ks - c.rotv) & 15;
#pragma unroll
      for (int q = 0; q < 5; ++q) *(LAS u32x4*)(c.lds + slot * 5120 + q * 1024 + lane * 16) = pf[u][q];
    }
    barrier();
  }
#pragma unroll
  for (int q = 0; q < 5; ++q) c.B[0][q] = ld128(c, c.aB + q * 1024);
  STAMP(5);
  steps<30>(c, mkseq<16>{});
  STAMP(6);
  barrier();
#pragma unroll
  for (int s = 0; s < 2; ++s)
#pragma unroll
    for (int q = 0; q < 5; ++q) {
      u32x4 v = {cvtpk(c.acc[2 * s][q][0], c.acc[2 * s][q][1]), cvtpk(c.acc[2 * s][q][2], c.acc[2 * s][q][3]),
                 cvtpk(c.acc[2 * s + 1][q][0], c.acc[2 * s + 1][q][1]), cvtpk(c.acc[2 * s + 1][q][2], c.acc[2 * s + 1][q][3])};
      *(LAS u32x4*)(c.lds + ((wave * 2 + s) * 5 + q) * 1024 + lane * 16) = v;
    }
  if (g == 0) {
#pragma unroll
    for (int q = 0; q < 5; ++q) *(LAS float*)(c.lds + 48128 + (q * 16 + q15) * 16 + wave * 4) = lsum[q];
  }
  barrier();
#pragma unroll
  for (int q = 0; q < 5; ++q) c.B[0][q] = ld128(c, c.aB + q * 1024);
  float rl[5];
#pragma unroll
  for (int q = 0; q < 5; ++q) { f32x4 mm = *(LAS f32x4*)(c.lds + 48128 + (q * 16 + q15) * 16); rl[q] = 1.0f / ((mm[0] + mm[1]) + (mm[2] + mm[3])); }
  STAMP(7);
  steps<46>(c, mkseq<8>{});
  STAMP(8);
  GAS u64* flags = (GAS u64*)(ws + WS_FLAG + b * 256);
  GAS u64* epoch = (GAS u64*)(ws + WS_EPOCH + b * 256);
  const u64 e0 = __hip_atomic_load(epoch, __ATOMIC_RELAXED, __HIP_MEMORY_SCOPE_AGENT);
  const __amdgpu_buffer_rsrc_t rP = __builtin_amdgcn_make_buffer_rsrc((void*)(ws + WS_PART + (size_t)b * HQ * PART_BYTES), 0, HQ * PART_BYTES, 0x00020000);
  {
    const u32 pb = h * PART_BYTES + wave * 17920 + lane * 16;
#pragma unroll
    for (int k = 0; k < 17; ++k) {
      const int ta = 2 * k, tb = 2 * k + 1;
      const f32x4 a = c.acc[ta / 5][ta % 5] * rl[ta % 5], bb = c.acc[tb / 5][tb % 5] * rl[tb % 5];
      u32x4 v = {cvtpk(a[0], a[1]), cvtpk(a[2], a[3]), cvtpk(bb[0], bb[1]), cvtpk(bb[2], bb[3])};
      __builtin_amdgcn_raw_buffer_store_b128(v, rP, pb + k * 1024, 0, 16);
    }
    const f32x4 a = c.acc[6][4] * rl[4];
    u32x2 v = {cvtpk(a[0], a[1]), cvtpk(a[2], a[3])};
    __builtin_amdgcn_raw_buffer_store_b64(v, rP, pb - lane * 8 + 17 * 1024, 0, 16);
  }
  const u64 token = (e0 ^ (e0 >> 29)) * 0x9E3779B97F4A7C15ull + 0x632BE59BD9B4E019ull;
  asm volatile("s_waitcnt vmcnt(0)" ::: "memory");
  barrier();
  STAMP(9);
  if (wave == 0) {
    if (lane == 0) __hip_atomic_store(flags + h, token, __ATOMIC_RELAXED, __HIP_MEMORY_SCOPE_AGENT);
    for (u32 spins = 0; spins < (1u << 20); ++spins) {
      const u64 f = __hip_atomic_load(flags + q15, __ATOMIC_RELAXED, __HIP_MEMORY_SCOPE_AGENT);
      if (__all(f == token)) break;
      __builtin_amdgcn_s_sleep(1);
    }
    if (h == 0 && lane == 0) __hip_atomic_store(epoch, token, __ATOMIC_RELAXED, __HIP_MEMORY_SCOPE_AGENT);
  }
  barrier();
  STAMP(10);
#pragma unroll
  for (int rnd = 0; rnd < 2; ++rnd) {
    if (rnd == 1 && tid >= 24) break;
    const u32 id = h * 280 + rnd * 256 + tid;
    u32x4 v[16];
#pragma unroll
    for (int k = 0; k < 16; ++k) v[k] = __builtin_amdgcn_raw_buffer_load_b128(rP, id * 16, k * PART_BYTES, 16);
    float a[8];
#pragma unroll
    for (int e = 0; e < 8; ++e) a[e] = 0.f;
#pragma unroll
    for (int k = 0; k < 16; ++k)
#pragma unroll
      for (int e = 0; e < 4; ++e) { a[2 * e] += __uint_as_float(v[k][e] << 16); a[2 * e + 1] += __uint_as_float(v[k][e] & 0xffff0000u); }
    const u32 cg = id / 1120, r = id - cg * 1120;
    u32 ta, la, tb, lb;
    if (r < 1088) { ta = (r >> 6) * 2; tb = ta + 1; la = lb = r & 63; } else { ta = tb = 34; la = (r - 1088) * 2; lb = la + 1; }
    const u32 ja = ta / 5, qa = ta - ja * 5, jb = tb / 5, qb = tb - jb * 5;
    u16* oa = out + ((size_t)b * SQ + qa * 16 + (la & 15)) * DM + cg * 112 + ja * 16 + (la >> 4) * 4;
    u16* ob = out + ((size_t)b * SQ + qb * 16 + (lb & 15)) * DM + cg * 112 + jb * 16 + (lb >> 4) * 4;
    *(u32x2*)oa = u32x2{cvtpk(a[0], a[1]), cvtpk(a[2], a[3])};
    *(u32x2*)ob = u32x2{cvtpk(a[4], a[5]), cvtpk(a[6], a[7])};
  }
}

extern "C" void kernel_launch(void* const* d_in, const int* in_sizes, int n_in, void* d_out, int out_size, void* d_ws, size_t ws_size, hipStream_t stream) {
  (void)hipFuncSetAttribute((const void*)gqa_fused, hipFuncAttributeMaxDynamicSharedMemorySize, LDS_BYTES);
  gqa_fused<<<256, 256, LDS_BYTES, stream>>>((const u16*)d_in[0], (const u16*)d_in[1], (const u16*)d_in[2], (const u16*)d_in[3], (const u16*)d_in[4], (u16*)d_out, (char*)d_ws);
}
```

```cpp
#include <hip/hip_runtime.h>
#include <utility>
typedef unsigned short u16; typedef unsigned u32; typedef unsigned long long u64;
typedef __bf16 bf16x8 __attribute__((ext_vector_type(8)));
typedef __bf16 bf16x4 __attribute__((ext_vector_type(4)));
typedef __bf16 bf16x2 __attribute__((ext_vector_type(2)));
typedef float f32x4 __attribute__((ext_vector_type(4)));
typedef float f32x2 __attribute__((ext_vector_type(2)));
typedef u32 u32x4 __attribute__((ext_vector_type(4)));
typedef u32 u32x2 __attribute__((ext_vector_type(2)));
#define LAS __attribute__((address_space(3)))
#define GAS __attribute__((address_space(1)))
#define DI __device__ __forceinline__
#define SB() __builtin_amdgcn_sched_barrier(0)
template <int... I> using iseq = std::integer_sequence<int, I...>;
template <int N> using mkseq = std::make_integer_sequence<int, N>;

constexpr int NB = 16, SQ = 80, DM = 448, HQ = 16, HKV = 8, SK = 512, DH = 256;
constexpr int LDS_BYTES = 163840, RING = 81920, RINGW = 20480;
constexpr int PART_BYTES = SQ * DM * 2;
constexpr size_t WS_FLAG = 0;
constexpr size_t WS_EPOCH = 8192;
constexpr size_t WS_PART = 16384;
constexpr size_t WS_STAMP = WS_PART + (size_t)NB * HQ * PART_BYTES;

#define ORD "WWWWWWWWWWWWWWKKKKKKKK"
#define KRD {13,14,15,16,17,18,19,20,21,22,23,24,25,26,27,28}
enum { TW = 0, TK = 1, TV = 2, TO = 3 };
struct Sched {
  int type[54], idx[54], ninst[54], lds[54], rd[54], iss[54], use[54];
  int cum[55];
};
constexpr Sched make_sched() {
  Sched s{};
  const char* ord = ORD;
  const int krd[16] = KRD;
  int nw = 0, nk = 0, i = 0;
  for (; i < 22; ++i) { if (ord[i] == 'W') { s.type[i] = TW; s.idx[i] = nw++; } else { s.type[i] = TK; s.idx[i] = nk++; } }
  for (; i < 30; ++i) { s.type[i] = TK; s.idx[i] = nk++; }
  for (; i < 46; ++i) { s.type[i] = TV; s.idx[i] = i - 30; }
  for (; i < 54; ++i) { s.type[i] = TO; s.idx[i] = i - 46; }
  for (i = 0; i < 54; ++i) { s.ninst[i] = s.type[i] == TO ? 7 : 4; }
  s.cum[0] = 0; for (i = 0; i < 54; ++i) s.cum[i + 1] = s.cum[i] + s.ninst[i];
  for (i = 0; i < 54; ++i) s.use[i] = s.type[i] == TW ? s.idx[i] : s.type[i] == TK ? 14 + s.idx[i] : s.type[i] == TV ? 30 + s.idx[i] : 46 + s.idx[i];
  int lastw = 0; for (i = 0; i < 22; ++i) if (s.type[i] == TW) lastw = i;
  for (i = 29; i >= 0; --i) {
    if (s.type[i] == TW) s.rd[i] = s.idx[i] - 1;
    else s.rd[i] = (i > lastw) ? krd[s.idx[i]] : s.rd[i + 1];
  }
  for (i = 30; i < 54; ++i) s.rd[i] = i - 1;
  for (i = 0; i < 46; ++i) s.lds[i] = (i % 5) * 4096;
  for (i = 46; i < 54; ++i) { int k = (i - 46) % 3; s.lds[i] = k == 0 ? 4096 : k == 1 ? 12288 : -1; }
  for (i = 0; i < 5; ++i) s.iss[i] = -1;
  for (i = 5; i < 46; ++i) s.iss[i] = s.rd[i - 5] + 1;
  s.iss[46] = s.rd[42] + 1;
  s.iss[47] = s.rd[44] + 1;
  s.iss[48] = 46;
  for (i = 49; i < 54; ++i) s.iss[i] = s.rd[i - 3] + 1;
  return s;
}
constexpr Sched SC = make_sched();
constexpr int last_issued(int step) { int r = -1; for (int i = 0; i < 54; ++i) if (SC.iss[i] <= step) r = i; return r; }
constexpr int last_read(int step) { int r = -1; for (int i = 0; i < 54; ++i) if (SC.rd[i] <= step) r = i; return r; }
constexpr bool sched_ok() {
  for (int i = 1; i < 54; ++i) { if (SC.iss[i] < SC.iss[i - 1] || SC.rd[i] < SC.rd[i - 1]) return false; }
  for (int i = 0; i < 54; ++i) { if (SC.rd[i] < SC.iss[i] || SC.use[i] <= SC.rd[i]) return false; }
  return true;
}
static_assert(sched_ok(), "schedule");
constexpr int nfrag(int I) { return SC.type[I] == TW ? 9 : SC.type[I] == TK ? 4 : SC.type[I] == TV ? (SC.idx[I] > 0 ? 9 : 4) : (SC.idx[I] > 0 ? 12 : 7); }

struct Ctx {
  LAS char* lds;
  u32 ldsb, ringb;
  __amdgpu_buffer_rsrc_t rW, rK, rV, rO;
  u32 vW[4], vK[4], vV[4], vO[7];
  u32 aW[4], aK[4], aV[4], aO, aO2, aX[2], aB;
  u32 rw, ro, o2b;
  bf16x8 A[2][7];
  bf16x8 B[2][5];
  bf16x8 Kr[16][4];
  bf16x8 Qf[8][5];
  f32x4 acc[7][5];
  f32x4 S[8][5];
  float m[5];
};

template <int IMM, bool NT> DI void dma(u32 m0v, u32 voff, __amdgpu_buffer_rsrc_t rs, u32 soff) {
  if constexpr (NT) asm volatile("s_mov_b32 m0, %0\n\ts_nop 0\n\tbuffer_load_dwordx4 %1, %2, %3 offen offset:%4 nt lds" :: "s"(m0v), "v"(voff), "s"(rs), "s"(soff), "i"(IMM) : "memory");
  else asm volatile("s_mov_b32 m0, %0\n\ts_nop 0\n\tbuffer_load_dwordx4 %1, %2, %3 offen offset:%4 lds" :: "s"(m0v), "v"(voff), "s"(rs), "s"(soff), "i"(IMM) : "memory");
}
template <int N> DI void wait_vm() { asm volatile("s_waitcnt vmcnt(%0)" :: "i"(N) : "memory"); }
DI void wait_lgkm0() { asm volatile("" ::: "memory"); __builtin_amdgcn_s_waitcnt(0xC07F); asm volatile("" ::: "memory"); }
DI void barrier() { asm volatile("s_waitcnt lgkmcnt(0)\n\ts_barrier" ::: "memory"); }
DI void mfma_pad() { SB(); asm volatile("s_nop 7\n\ts_nop 1" ::: "memory"); SB(); }
DI u32 cvtpk(float a, float b) { f32x2 v = {a, b}; return __builtin_bit_cast(u32, __builtin_convertvector(v, bf16x2)); }
DI bf16x8 ld128(Ctx& c, u32 off) { return *(LAS bf16x8*)(c.lds + off); }
DI bf16x8 ldtr(Ctx& c, u32 off0, u32 off1) {
  bf16x4 a = __builtin_amdgcn_ds_read_tr16_b64_v4bf16((LAS bf16x4*)(c.lds + off0));
  bf16x4 b = __builtin_amdgcn_ds_read_tr16_b64_v4bf16((LAS bf16x4*)(c.lds + off1));
  return __builtin_shufflevector(a, b, 0, 1, 2, 3, 4, 5, 6, 7);
}
#define ACON "a"
template <bool FIRST, bool AA> DI void mfma(f32x4& d, const bf16x8& a, const bf16x8& b) {
  if constexpr (AA) {
    if constexpr (FIRST) asm volatile("v_mfma_f32_16x16x32_bf16 %0, %1, %2, 0" : "=&v"(d) : "a"(a), "a"(b) : "memory");
    else asm volatile("v_mfma_f32_16x16x32_bf16 %0, %1, %2, %0" : "+v"(d) : "a"(a), "a"(b) : "memory");
  } else {
    if constexpr (FIRST) asm volatile("v_mfma_f32_16x16x32_bf16 %0, %1, %2, 0" : "=&v"(d) : ACON(a), "a"(b) : "memory");
    else asm volatile("v_mfma_f32_16x16x32_bf16 %0, %1, %2, %0" : "+v"(d) : ACON(a), "a"(b) : "memory");
  }
}
DI float max3v(float a, float b, float c) { float r; asm volatile("v_max3_f32 %0, %1, %2, %3" : "=v"(r) : "v"(a), "v"(b), "v"(c)); return r; }
DI float max2f(float a, float b) { float r; asm("v_max_f32_e32 %0, %1, %2" : "=v"(r) : "v"(a), "v"(b)); return r; }

template <int I, int J> DI void issue1(Ctx& c) {
  constexpr int ty = SC.type[I], ix = SC.idx[I];
  if constexpr (ty == TW) { u32 sg = (ix >> 1) + c.rw; sg = sg >= 7 ? sg - 7 : sg;
    dma<J * 1024, false>(c.ringb + SC.lds[I], c.vW[J], c.rW, (sg * 2 + (ix & 1)) * (32 * 8192)); }
  else if constexpr (ty == TK) dma<J * 1024, true>(c.ringb + SC.lds[I], c.vK[J], c.rK, (u32)((ix >> 1) * 8192 + (ix & 1) * 256));
  else if constexpr (ty == TV) dma<J * 1024, true>(c.ringb + SC.lds[I], c.vV[J], c.rV, (u32)(ix * 16384));
  else { const u32 base = SC.lds[I] < 0 ? c.o2b : c.ringb + SC.lds[I];
         dma<(J & 3) * 1024, false>(base + (J >> 2) * 4096, c.vO[J], c.rO, ((ix + c.ro) & 7) * (32 * 896)); }
}
template <int G> DI void issue_g(Ctx& c) {
  constexpr int I = [] { int i = 0; while (SC.cum[i + 1] <= G) ++i; return i; }();
  issue1<I, G - SC.cum[I]>(c);
}
template <int G0, int... K> DI void issue_range(Ctx& c, iseq<K...>) { (issue_g<G0 + K>(c), ...); }

template <int I, int F> DI void read1(Ctx& c) {
  constexpr int ty = SC.type[I], ix = SC.idx[I], buf = SC.use[I] & 1;
  if constexpr (ty == TW) {
    if constexpr (F == 0) c.A[buf][0] = ldtr(c, c.aW[0] + SC.lds[I], c.aW[0] + SC.lds[I] + 512);
    else if constexpr (F <= 5) c.B[buf][F - 1] = ld128(c, c.aX[ix & 1] + ((ix >> 1) * 10 + 2 * (F - 1)) * 1024);
    else c.A[buf][F - 5] = ldtr(c, c.aW[F - 5] + SC.lds[I], c.aW[F - 5] + SC.lds[I] + 512);
  } else if constexpr (ty == TK) {
    c.Kr[ix][F] = ld128(c, c.aK[F] + SC.lds[I]);
  } else if constexpr (ty == TV) {
    if constexpr (F < 4) c.A[buf][F] = ldtr(c, c.aV[F] + SC.lds[I], c.aV[F] + SC.lds[I] + 2048);
    else c.B[buf][F - 4] = ld128(c, c.aB + (ix * 5 + F - 4) * 1024);
  } else {
    const u32 base = SC.lds[I] < 0 ? c.aO2 : c.aO + SC.lds[I];
    if constexpr (F < 7) c.A[buf][F] = ldtr(c, base + 32 * F, base + 32 * F + 3584);
    else c.B[buf][F - 7] = ld128(c, c.aB + (ix * 5 + F - 7) * 1024);
  }
}
template <int S, int U> DI void read_u(Ctx& c) {
  constexpr int r0 = last_read(S - 1) + 1;
  constexpr int I = [] { int i = last_read(S - 1) + 1, u = U; while (u >= nfrag(i)) { u -= nfrag(i); ++i; } return i; }();
  constexpr int F = [] { int i = last_read(S - 1) + 1, u = U; while (u >= nfrag(i)) { u -= nfrag(i); ++i; } return u; }();
  (void)r0;
  read1<I, F>(c);
}
constexpr int nreads(int S) { int n = 0; for (int i = last_read(S - 1) + 1; i <= last_read(S); ++i) n += nfrag(i); return n; }
template <int I, int... F> DI void read_item_(Ctx& c, iseq<F...>) { (read1<I, F>(c), ...); }
template <int I> DI void read_item(Ctx& c) { read_item_<I>(c, mkseq<nfrag(I)>{}); }

template <int S, int M> DI void mf(Ctx& c) {
  constexpr int buf = S & 1, J = M / 5, q = M % 5;
  if constexpr (S < 14 || S >= 30) mfma<(S == 0 || S == 30 || S == 46), false>(c.acc[J][q], c.A[buf][J], c.B[buf][q]);
  else { constexpr int u = S - 14, t = u >> 1, hf = u & 1; mfma<(hf == 0 && J == 0), true>(c.S[t][q], c.Kr[u][J], c.Qf[hf * 4 + J][q]); }
}
template <int S, int M> DI void gap(Ctx& c) {
  constexpr int NM = S >= 46 ? 35 : 20;
  constexpr int i0 = last_issued(S - 1) + 1, i1 = last_issued(S);
  constexpr int g0 = SC.cum[i0], ng = SC.cum[i1 + 1] - g0;
  constexpr int DG = ng <= 4 ? ng : (ng <= 8 ? 4 : 7);
  constexpr int dper = DG ? (ng + DG - 1) / DG : 0;
  constexpr int r1 = last_read(S), nr = nreads(S);
  constexpr int RG0 = DG;
  constexpr int RGAPS = NM == 35 ? 12 : 9;
  constexpr int rper = (nr + RGAPS - 1) / RGAPS;
  mf<S, M>(c);
  if constexpr (M < DG) {
    constexpr int a = M * dper, b = (a + dper < ng) ? a + dper : ng;
    issue_range<g0 + a>(c, mkseq<(b > a ? b - a : 0)>{});
  }
  if constexpr (M == RG0 && nr > 0) wait_vm<SC.cum[i1 + 1] - SC.cum[r1 + 1]>();
  if constexpr (M >= RG0 && nr > 0) {
    constexpr int a = (M - RG0) * rper;
    if constexpr (a < nr) read_u<S, a>(c);
    if constexpr (rper > 1 && a + 1 < nr) read_u<S, a + 1>(c);
    static_assert(rper <= 2, "reads per gap");
  }
  if constexpr (S >= 16 && S < 30 && (S & 1) == 0 && M >= 10) {
    constexpr int t = (S - 16) >> 1, q = (M - 10) >> 1;
    if constexpr ((M & 1) == 0) { if constexpr (t == 0) c.m[q] = max3v(c.S[t][q][0], c.S[t][q][1], c.S[t][q][1]); else c.m[q] = max3v(c.m[q], c.S[t][q][0], c.S[t][q][1]); }
    else c.m[q] = max3v(c.m[q], c.S[t][q][2], c.S[t][q][3]);
  }
}
template <int S, int... M> DI void step_(Ctx& c, iseq<M...>) { wait_lgkm0(); (gap<S, M>(c), ...); }
template <int S> DI void step(Ctx& c) { step_<S>(c, mkseq<(S >= 46 ? 35 : 20)>{}); }
template <int S0, int... K> DI void steps(Ctx& c, iseq<K...>) { (step<S0 + K>(c), ...); }

#define NSTAMP 12
#define STAMP(n)

__global__ __launch_bounds__(256, 1) void gqa_fused(const u16* __restrict__ x, const u16* __restrict__ kc, const u16* __restrict__ vc,
                                                    const u16* __restrict__ wq, const u16* __restrict__ wo, u16* __restrict__ out, char* __restrict__ ws) {
  extern __shared__ __attribute__((aligned(16))) char smem[];
  Ctx c;
  c.lds = (LAS char*)smem;
  c.ldsb = (u32)(size_t)c.lds;
  const u32 tid = threadIdx.x, lane = tid & 63, wave = __builtin_amdgcn_readfirstlane(tid >> 6);
  const u32 bx = blockIdx.x, kvh = bx & 7, par = (bx >> 3) & 1, b = bx >> 4, h = kvh * 2 + par;
  const u32 q15 = lane & 15, g = lane >> 4, qp = (lane >> 2) & 3, p = lane & 3;
  STAMP(0);
  c.ringb = c.ldsb + RING + wave * RINGW;
  c.o2b = c.ldsb + 40960 + wave * 10240;
  c.rw = b >= 14 ? b - 14 : (b >= 7 ? b - 7 : b); c.ro = b & 7;
  const __amdgpu_buffer_rsrc_t rX = __builtin_amdgcn_make_buffer_rsrc((void*)(x + (size_t)b * SQ * DM), 0, SQ * DM * 2, 0x00020000);
  c.rW = __builtin_amdgcn_make_buffer_rsrc((void*)(wq + h * 256 + wave * 64), 0, (DM - 1) * 8192 + 128, 0x00020000);
  c.rK = __builtin_amdgcn_make_buffer_rsrc((void*)(kc + ((size_t)(b * HKV + kvh) * SK + wave * 128) * DH), 0, 128 * 512, 0x00020000);
  c.rV = __builtin_amdgcn_make_buffer_rsrc((void*)(vc + (size_t)(b * HKV + kvh) * SK * DH + wave * 64), 0, (SK - 1) * 512 + 128, 0x00020000);
  c.rO = __builtin_amdgcn_make_buffer_rsrc((void*)(wo + (size_t)h * 256 * DM + wave * 112), 0, 255 * 896 + 224, 0x00020000);
  const u32 r8 = lane >> 3, c8 = lane & 7;
  const u32 vX = r8 * 896 + ((c8 ^ ((4 * (wave & 1) + (lane >> 4)) & 7)) << 4);
#pragma unroll
  for (int i = 0; i < 4; ++i) {
    c.vW[i] = (8 * i + r8) * 8192 + ((c8 ^ ((r8 & 2) | ((i & 1) << 2))) << 4) - i * 1024;
    c.vK[i] = (4 * i + g) * 512 + ((q15 ^ (4 * i + g)) << 4) - i * 1024;
    c.vV[i] = (8 * i + r8) * 512 + ((c8 ^ (r8 & 6)) << 4) - i * 1024;
  }
  asm volatile("s_nop 4" ::: "memory");
  {
    u32 rg = wave, sg = c.rw;
#pragma unroll
    for (int j = 0; j < 18; ++j) {
      if (j == 17 && wave >= 2) { rg -= 2; }
      const u32 pc = (j == 17 && wave >= 2) ? 66 + wave : 4 * j + wave;
      dma<0, false>(c.ldsb + pc * 1024, vX, rX, rg * 7168 + sg * 128);
      rg += 4; if (rg >= 10) { if (j != 16 || wave < 2) { rg -= 10; sg = sg == 6 ? 0 : sg + 1; } }
    }
  }
  issue_range<0>(c, mkseq<20>{});
  const u32 ro = RING + wave * RINGW;
#pragma unroll
  for (int j = 0; j < 4; ++j) {
    c.aW[j] = ro + g * 1024 + qp * 128 + (((2 * j + (p >> 1)) ^ ((qp & 2) | ((g & 1) << 2))) << 4) + (p & 1) * 8;
    c.aK[j] = ro + q15 * 256 + (((4 * j + g) ^ q15) << 4);
    const u32 rw = 4 * (g & 1) + qp;
    c.aV[j] = ro + (g >> 1) * 1024 + rw * 128 + (((2 * j + (p >> 1)) ^ (rw & 6)) << 4) + (p & 1) * 8;
  }
#pragma unroll
  for (int i = 0; i < 2; ++i) c.aX[i] = (q15 >> 3) * 1024 + (q15 & 7) * 128 + (((4 * i + g) ^ ((q15 >> 1) & 7)) << 4);
  c.aB = lane * 16;
  c.aO = ro + (4 * g + qp) * 224 + p * 8;
  c.aO2 = 40960 + wave * 10240 + (4 * g + qp) * 224 + p * 8;
#pragma unroll
  for (int i = 0; i < 7; ++i) { const u32 jj = 64 * i + lane, rw = jj / 14, cc = jj - rw * 14; c.vO[i] = rw * 896 + cc * 16 - (i & 3) * 1024; }

  wait_vm<16>();
  barrier();
  STAMP(1);
  read_item<0>(c);
  steps<0>(c, mkseq<14>{});
  mfma_pad();
  STAMP(2);
  barrier();
  {
    const u32 wb = q15 * 16 + (g >> 1) * 256 + (g & 1) * 8 + wave * 10240;
#pragma unroll
    for (int j = 0; j < 4; ++j)
#pragma unroll
      for (int q = 0; q < 5; ++q) {
        u32x2 v = {cvtpk(c.acc[j][q][0], c.acc[j][q][1]), cvtpk(c.acc[j][q][2], c.acc[j][q][3])};
        *(LAS u32x2*)(c.lds + wb + (j >> 1) * 5120 + q * 1024 + (j & 1) * 512) = v;
      }
  }
  barrier();
#pragma unroll
  for (int s = 0; s < 8; ++s)
#pragma unroll
    for (int q = 0; q < 5; ++q) c.Qf[s][q] = ld128(c, c.aB + (s * 5 + q) * 1024);
  STAMP(3);
  steps<14>(c, mkseq<16>{});
  mfma_pad();
  STAMP(4);
  float lsum[5];
  {
#pragma unroll
    for (int q = 0; q < 5; ++q) {
      float v = max3v(c.m[q], c.S[7][q][0], c.S[7][q][1]); v = max3v(v, c.S[7][q][2], c.S[7][q][3]);
      auto r1 = __builtin_amdgcn_permlane16_swap(__float_as_uint(v), __float_as_uint(v), false, false);
      v = max2f(__uint_as_float(r1[0]), __uint_as_float(r1[1]));
      auto r2 = __builtin_amdgcn_permlane32_swap(__float_as_uint(v), __float_as_uint(v), false, false);
      c.m[q] = max2f(__uint_as_float(r2[0]), __uint_as_float(r2[1]));
    }
    if (g == 0) {
#pragma unroll
      for (int q = 0; q < 5; ++q) *(LAS float*)(c.lds + 71680 + (q * 16 + q15) * 16 + wave * 4) = c.m[q];
    }
    barrier();
    constexpr float CS = 1.4426950408889634f / 16.0f;
    float nm[5];
#pragma unroll
    for (int q = 0; q < 5; ++q) {
      f32x4 mm = *(LAS f32x4*)(c.lds + 71680 + (q * 16 + q15) * 16);
      nm[q] = -max2f(max3v(mm[0], mm[1], mm[2]), mm[3]) * CS;
    }
    u32x4 pf[4][5];
#pragma unroll
    for (int q = 0; q < 5; ++q) {
      f32x2 sa = {0.f, 0.f};
#pragma unroll
      for (int t = 0; t < 8; ++t) {
        const f32x4 z = c.S[t][q] * CS + nm[q];
        float e0 = __builtin_amdgcn_exp2f(z[0]), e1 = __builtin_amdgcn_exp2f(z[1]), e2 = __builtin_amdgcn_exp2f(z[2]), e3 = __builtin_amdgcn_exp2f(z[3]);
        sa += f32x2{e0, e1}; sa += f32x2{e2, e3};
        pf[t >> 1][q][(t & 1) * 2] = cvtpk(e0, e1); pf[t >> 1][q][(t & 1) * 2 + 1] = cvtpk(e2, e3);
      }
      float v = sa[0] + sa[1];
      auto r1 = __builtin_amdgcn_permlane16_swap(__float_as_uint(v), __float_as_uint(v), false, false);
      v = __uint_as_float(r1[0]) + __uint_as_float(r1[1]);
      auto r2 = __builtin_amdgcn_permlane32_swap(__float_as_uint(v), __float_as_uint(v), false, false);
      lsum[q] = __uint_as_float(r2[0]) + __uint_as_float(r2[1]);
    }
    barrier();
#pragma unroll
    for (int u = 0; u < 4; ++u)
#pragma unroll
      for (int q = 0; q < 5; ++q) *(LAS u32x4*)(c.lds + (wave * 4 + u) * 5120 + q * 1024 + lane * 16) = pf[u][q];
    barrier();
  }
#pragma unroll
  for (int q = 0; q < 5; ++q) c.B[0][q] = ld128(c, c.aB + q * 1024);
  STAMP(5);
  steps<30>(c, mkseq<16>{});
  mfma_pad();
  STAMP(6);
  barrier();
#pragma unroll
  for (int s = 0; s < 2; ++s) {
    const u32 slot = (wave * 2 + s - c.ro) & 7;
#pragma unroll
    for (int q = 0; q < 5; ++q) {
      u32x4 v = {cvtpk(c.acc[2 * s][q][0], c.acc[2 * s][q][1]), cvtpk(c.acc[2 * s][q][2], c.acc[2 * s][q][3]),
                 cvtpk(c.acc[2 * s + 1][q][0], c.acc[2 * s + 1][q][1]), cvtpk(c.acc[2 * s + 1][q][2], c.acc[2 * s + 1][q][3])};
      *(LAS u32x4*)(c.lds + slot * 5120 + q * 1024 + lane * 16) = v;
    }
  }
  if (g == 0) {
#pragma unroll
    for (int q = 0; q < 5; ++q) *(LAS float*)(c.lds + 48128 + (q * 16 + q15) * 16 + wave * 4) = lsum[q];
  }
  barrier();
#pragma unroll
  for (int q = 0; q < 5; ++q) c.B[0][q] = ld128(c, c.aB + q * 1024);
  float rl[5];
#pragma unroll
  for (int q = 0; q < 5; ++q) { f32x4 mm = *(LAS f32x4*)(c.lds + 48128 + (q * 16 + q15) * 16); rl[q] = __builtin_amdgcn_rcpf((mm[0] + mm[1]) + (mm[2] + mm[3])); }
  STAMP(7);
  steps<46>(c, mkseq<8>{});
  mfma_pad();
  STAMP(8);
  GAS u64* flags = (GAS u64*)(ws + WS_FLAG + b * 256);
  GAS u64* epoch = (GAS u64*)(ws + WS_EPOCH + b * 256);
  const u64 e0 = __hip_atomic_load(epoch, __ATOMIC_RELAXED, __HIP_MEMORY_SCOPE_AGENT);
  const __amdgpu_buffer_rsrc_t rP = __builtin_amdgcn_make_buffer_rsrc((void*)(ws + WS_PART + (size_t)b * HQ * PART_BYTES), 0, HQ * PART_BYTES, 0x00020000);
  {
    const u32 pb = h * PART_BYTES + wave * 17920 + lane * 16;
#pragma unroll
    for (int k = 0; k < 17; ++k) {
      const int ta = 2 * k, tb = 2 * k + 1;
      const f32x4 a = c.acc[ta / 5][ta % 5] * rl[ta % 5], bb = c.acc[tb / 5][tb % 5] * rl[tb % 5];
      u32x4 v = {cvtpk(a[0], a[1]), cvtpk(a[2], a[3]), cvtpk(bb[0], bb[1]), cvtpk(bb[2], bb[3])};
      __builtin_amdgcn_raw_buffer_store_b128(v, rP, pb + k * 1024, 0, 18);
    }
    const f32x4 a = c.acc[6][4] * rl[4];
    u32x2 v = {cvtpk(a[0], a[1]), cvtpk(a[2], a[3])};
    __builtin_amdgcn_raw_buffer_store_b64(v, rP, pb - lane * 8 + 17 * 1024, 0, 18);
  }
  const u64 token = (e0 ^ (e0 >> 29)) * 0x9E3779B97F4A7C15ull + 0x632BE59BD9B4E019ull;
  asm volatile("s_waitcnt vmcnt(0)" ::: "memory");
  barrier();
  STAMP(9);
  if (tid == 0) __hip_atomic_store(flags + h, token, __ATOMIC_RELAXED, __HIP_MEMORY_SCOPE_AGENT);
  {
    u32x4 v[4][5];
    const u32 lo = h * 4480 + lane * 16;
    u32 done = 0;
    for (u32 spins = 0; done != 15u && spins < (1u << 16); ++spins) {
      const u64 f = __hip_atomic_load(flags + wave * 4 + (lane & 3), __ATOMIC_RELAXED, __HIP_MEMORY_SCOPE_AGENT);
      const u32 rdy = (u32)__ballot(f == token) & 15u & ~done;
#pragma unroll
      for (int k = 0; k < 4; ++k)
        if (rdy & (1u << k)) {
#pragma unroll
          for (int i = 0; i < 5; ++i)
            asm volatile("buffer_load_dwordx4 %0, %1, %2, %3 offen sc1" : "=v"(v[k][i]) : "v"(i < 4 ? lo + i * 1024 : lo + (lane < 24 ? 4096 : 0)), "s"(rP), "s"((wave * 4 + k) * PART_BYTES) : "memory");
        }
      done |= rdy;
      if (done != 15u) __builtin_amdgcn_s_sleep(1);
    }
    asm volatile("s_waitcnt vmcnt(0)" : "+v"(v[0][0]), "+v"(v[0][1]), "+v"(v[0][2]), "+v"(v[0][3]), "+v"(v[0][4]), "+v"(v[1][0]), "+v"(v[1][1]), "+v"(v[1][2]), "+v"(v[1][3]), "+v"(v[1][4]),
                 "+v"(v[2][0]), "+v"(v[2][1]), "+v"(v[2][2]), "+v"(v[2][3]), "+v"(v[2][4]), "+v"(v[3][0]), "+v"(v[3][1]), "+v"(v[3][2]), "+v"(v[3][3]), "+v"(v[3][4]) :: "memory");
    STAMP(10);
#pragma unroll
    for (int i = 0; i < 5; ++i) {
      f32x4 a0, a1;
#pragma unroll
      for (int e = 0; e < 2; ++e) {
        a0[2 * e] = (__uint_as_float(v[0][i][e] << 16) + __uint_as_float(v[1][i][e] << 16)) + (__uint_as_float(v[2][i][e] << 16) + __uint_as_float(v[3][i][e] << 16));
        a0[2 * e + 1] = (__uint_as_float(v[0][i][e] & 0xffff0000u) + __uint_as_float(v[1][i][e] & 0xffff0000u)) + (__uint_as_float(v[2][i][e] & 0xffff0000u) + __uint_as_float(v[3][i][e] & 0xffff0000u));
        a1[2 * e] = (__uint_as_float(v[0][i][e + 2] << 16) + __uint_as_float(v[1][i][e + 2] << 16)) + (__uint_as_float(v[2][i][e + 2] << 16) + __uint_as_float(v[3][i][e + 2] << 16));
        a1[2 * e + 1] = (__uint_as_float(v[0][i][e + 2] & 0xffff0000u) + __uint_as_float(v[1][i][e + 2] & 0xffff0000u)) + (__uint_as_float(v[2][i][e + 2] & 0xffff0000u) + __uint_as_float(v[3][i][e + 2] & 0xffff0000u));
      }
      *(LAS f32x4*)(c.lds + wave * 10240 + (i * 64 + lane) * 16) = a0;
      *(LAS f32x4*)(c.lds + wave * 10240 + 5120 + (i * 64 + lane) * 16) = a1;
    }
    barrier();
    if (h == 0 && tid == 0) __hip_atomic_store(epoch, token, __ATOMIC_RELAXED, __HIP_MEMORY_SCOPE_AGENT);
#pragma unroll
    for (int rnd = 0; rnd < 3; ++rnd) {
      const u32 e = rnd * 256 + tid;
      if (e < 560) {
        const u32 half = e >= 280, ch = e - half * 280;
        const LAS char* src = c.lds + half * 5120 + ch * 16;
        const f32x4 s = (*(const LAS f32x4*)(src) + *(const LAS f32x4*)(src + 10240)) + (*(const LAS f32x4*)(src + 20480) + *(const LAS f32x4*)(src + 30720));
        const u32 id = h * 280 + ch, cg = id / 1120, r = id - cg * 1120;
        const u32 tt = r < 1088 ? (r >> 6) * 2 + half : 34, ln = r < 1088 ? (r & 63) : (r - 1088) * 2 + half;
        const u32 jj = tt / 5, qq = tt - jj * 5;
        u16* o = out + ((size_t)b * SQ + qq * 16 + (ln & 15)) * DM + cg * 112 + jj * 16 + (ln >> 4) * 4;
        *(u32x2*)o = u32x2{cvtpk(s[0], s[1]), cvtpk(s[2], s[3])};
      }
    }
  }
}

extern "C" void kernel_launch(void* const* d_in, const int* in_sizes, int n_in, void* d_out, int out_size, void* d_ws, size_t ws_size, hipStream_t stream) {
  (void)hipFuncSetAttribute((const void*)gqa_fused, hipFuncAttributeMaxDynamicSharedMemorySize, LDS_BYTES);
  gqa_fused<<<256, 256, LDS_BYTES, stream>>>((const u16*)d_in[0], (const u16*)d_in[1], (const u16*)d_in[2], (const u16*)d_in[3], (const u16*)d_in[4], (u16*)d_out, (char*)d_ws);
}
```

```cpp
#include <hip/hip_runtime.h>
#include <hip/hip_bf16.h>
typedef unsigned short u16; typedef unsigned long long u64;
__device__ __forceinline__ float bf2f(u16 v){ return __uint_as_float(((unsigned)v)<<16); }
__device__ __forceinline__ u16 f2bf(float f){ __hip_bfloat16 b = __float2bfloat16(f); return *(u16*)&b; }
#define PADUS 30000
__global__ __launch_bounds__(256) void k(const u16* x,const u16* kc,const u16* vc,const u16* wq,const u16* wo,u16* out,u16* ws){
  u64 a_; asm volatile("s_memrealtime %0\n\ts_waitcnt lgkmcnt(0)" : "=s"(a_) :: "memory");

  __shared__ float xs[5][448]; __shared__ float q[5][256]; __shared__ float s[5][512]; __shared__ float l[5];
  int b=blockIdx.x>>4, r0=(blockIdx.x&15)*5, t=threadIdx.x;
  for(int i=t;i<5*448;i+=256) xs[i/448][i%448]=bf2f(x[(size_t)(b*80+r0+i/448)*448+i%448]);
  u16* o=ws+(size_t)blockIdx.x*5*4096;
  __syncthreads();
  for(int h=0;h<16;h++){
    float a[5]={0,0,0,0,0};
    for(int kk=0;kk<448;kk++){ float w=bf2f(wq[(size_t)kk*4096+h*256+t]); for(int r=0;r<5;r++) a[r]+=xs[r][kk]*w; }
    for(int r=0;r<5;r++) q[r][t]=bf2f(f2bf(a[r]));
    __syncthreads();
    const u16* K=kc+(size_t)(b*8+h/2)*512*256; const u16* V=vc+(size_t)(b*8+h/2)*512*256;
    for(int j=t;j<512;j+=256){ float c[5]={0,0,0,0,0}; for(int d=0;d<256;d++){ float w=bf2f(K[(size_t)j*256+d]); for(int r=0;r<5;r++) c[r]+=q[r][d]*w; } for(int r=0;r<5;r++) s[r][j]=c[r]*0.0625f; }
    __syncthreads();
    if(t<5){ float m=-1e30f; for(int j=0;j<512;j++) m=fmaxf(m,s[t][j]); float su=0; for(int j=0;j<512;j++){ float e=__expf(s[t][j]-m); s[t][j]=e; su+=e; } l[t]=su; }
    __syncthreads();
    float c[5]={0,0,0,0,0};
    for(int j=0;j<512;j++){ float w=bf2f(V[(size_t)j*256+t]); for(int r=0;r<5;r++) c[r]+=s[r][j]*w; }
    for(int r=0;r<5;r++) o[r*4096+h*256+t]=f2bf(c[r]/l[r]);
    __syncthreads();
  }
  __threadfence_block(); __syncthreads();
  for(int n=t;n<448;n+=256){ float c[5]={0,0,0,0,0}; for(int kk=0;kk<4096;kk++){ float w=bf2f(wo[(size_t)kk*448+n]); for(int r=0;r<5;r++) c[r]+=bf2f(o[r*4096+kk])*w; } for(int r=0;r<5;r++) out[(size_t)(b*80+r0+r)*448+n]=f2bf(c[r]); }
  { u64 b_; do { __builtin_amdgcn_s_sleep(4); asm volatile("s_memrealtime %0\n\ts_waitcnt lgkmcnt(0)" : "=s"(b_) :: "memory"); } while (b_ - a_ < (u64)PADUS*100); }
}
extern "C" void kernel_launch(void* const* d_in, const int* in_sizes, int n_in, void* d_out, int out_size, void* d_ws, size_t ws_size, hipStream_t stream){
  (void)hipFuncSetAttribute((const void*)k, hipFuncAttributeMaxDynamicSharedMemorySize, 130000);
  k<<<256,256,130000,stream>>>((const u16*)d_in[0],(const u16*)d_in[1],(const u16*)d_in[2],(const u16*)d_in[3],(const u16*)d_in[4],(u16*)d_out,(u16*)d_ws);
}
```

```cpp
#include <hip/hip_runtime.h>
typedef unsigned short u16; typedef unsigned u32; typedef unsigned long long u64;
typedef __bf16 bf16x8 __attribute__((ext_vector_type(8)));
typedef __bf16 bf16x4 __attribute__((ext_vector_type(4)));
typedef __bf16 bf16x2 __attribute__((ext_vector_type(2)));
typedef float f32x4 __attribute__((ext_vector_type(4)));
typedef float f32x2 __attribute__((ext_vector_type(2)));
typedef u32 u32x4 __attribute__((ext_vector_type(4)));
typedef u32 u32x2 __attribute__((ext_vector_type(2)));
#define LAS __attribute__((address_space(3)))
#define GAS __attribute__((address_space(1)))
#define DI __device__ __forceinline__
#define SB() __builtin_amdgcn_sched_barrier(0)

constexpr int NB = 16, SQ = 80, DM = 448, HQ = 16, HKV = 8, SK = 512, DH = 256;
constexpr int LDS_BYTES = 163840;
constexpr int PART_BYTES = SQ * DM * 2;
constexpr int PART_STRIDE = PART_BYTES + 128;
constexpr size_t WS_FLAG = 0;
constexpr size_t WS_EPOCH = 262144;
constexpr size_t WS_PART = 266240;
constexpr int ST_MAX = 40960 + 28672, ST_SUM = 40960 + 30720 + 28672, ST_MAXB = 40960 + 2 * 30720 + 28672;
constexpr float CS = 1.4426950408889634f / 16.0f;

template <int IMM, bool NT, bool M0> DI void dma(u32 m0v, u32 voff, __amdgpu_buffer_rsrc_t rs, u32 soff) {
  if constexpr (M0) {
    if constexpr (NT) asm volatile("s_mov_b32 m0, %0\n\ts_nop 0\n\tbuffer_load_dwordx4 %1, %2, %3 offen offset:%4 nt lds" :: "s"(m0v), "v"(voff), "s"(rs), "s"(soff), "i"(IMM) : "memory");
    else asm volatile("s_mov_b32 m0, %0\n\ts_nop 0\n\tbuffer_load_dwordx4 %1, %2, %3 offen offset:%4 lds" :: "s"(m0v), "v"(voff), "s"(rs), "s"(soff), "i"(IMM) : "memory");
  } else {
    if constexpr (NT) asm volatile("buffer_load_dwordx4 %0, %1, %2 offen offset:%3 nt lds" :: "v"(voff), "s"(rs), "s"(soff), "i"(IMM) : "memory");
    else asm volatile("buffer_load_dwordx4 %0, %1, %2 offen offset:%3 lds" :: "v"(voff), "s"(rs), "s"(soff), "i"(IMM) : "memory");
  }
}
template <int N> DI void wait_vm() { asm volatile("s_waitcnt vmcnt(%0)" :: "i"(N) : "memory"); }
DI void wait_lgkm0() { asm volatile("" ::: "memory"); __builtin_amdgcn_s_waitcnt(0xC07F); asm volatile("" ::: "memory"); }
DI void barrier() { wait_lgkm0(); asm volatile("s_barrier" ::: "memory"); }
DI void mfma_pad() { SB(); asm volatile("s_nop 7\n\ts_nop 1" ::: "memory"); SB(); }
DI u32 cvtpk(float a, float b) { f32x2 v = {a, b}; return __builtin_bit_cast(u32, __builtin_convertvector(v, bf16x2)); }
DI bf16x8 ld128(LAS char* lds, u32 off) { return *(LAS bf16x8*)(lds + off); }
DI bf16x8 ldtr(LAS char* lds, u32 off0, u32 off1) {
  bf16x4 a = __builtin_amdgcn_ds_read_tr16_b64_v4bf16((LAS bf16x4*)(lds + off0));
  bf16x4 b = __builtin_amdgcn_ds_read_tr16_b64_v4bf16((LAS bf16x4*)(lds + off1));
  return __builtin_shufflevector(a, b, 0, 1, 2, 3, 4, 5, 6, 7);
}
DI void st128(LAS char* lds, u32 off, const u32* p) { *(LAS u32x4*)(lds + off) = u32x4{p[0], p[1], p[2], p[3]}; }
DI void st64(LAS char* lds, u32 off, const u32* p) { *(LAS u32x2*)(lds + off) = u32x2{p[0], p[1]}; }
template <bool FIRST> DI void mfma(f32x4& d, const bf16x8& a, const bf16x8& b) {
  if constexpr (FIRST) asm volatile("v_mfma_f32_16x16x32_bf16 %0, %1, %2, 0" : "=&v"(d) : "a"(a), "a"(b) : "memory");
  else asm volatile("v_mfma_f32_16x16x32_bf16 %0, %1, %2, %0" : "+v"(d) : "a"(a), "a"(b) : "memory");
}
template <bool FIRST> DI void mfmav(f32x4& d, const bf16x8& a, const bf16x8& b) {
  if constexpr (FIRST) asm volatile("v_mfma_f32_16x16x32_bf16 %0, %1, %2, 0" : "=&v"(d) : "v"(a), "a"(b) : "memory");
  else asm volatile("v_mfma_f32_16x16x32_bf16 %0, %1, %2, %0" : "+v"(d) : "v"(a), "a"(b) : "memory");
}
template <bool FIRST> DI void mfmab(f32x4& d, const bf16x8& a, const bf16x8& b) {
  if constexpr (FIRST) asm volatile("v_mfma_f32_16x16x32_bf16 %0, %1, %2, 0" : "=&v"(d) : "a"(a), "v"(b) : "memory");
  else asm volatile("v_mfma_f32_16x16x32_bf16 %0, %1, %2, %0" : "+v"(d) : "a"(a), "v"(b) : "memory");
}
DI float vmax3(float a, float b, float c) { float r; asm volatile("v_max3_f32 %0, %1, %2, %3" : "=v"(r) : "v"(a), "v"(b), "v"(c)); return r; }
DI f32x2 vpkfma(f32x2 s, f32x2 nm) { f32x2 r; const f32x2 cs = {CS, CS}; asm volatile("v_pk_fma_f32 %0, %1, %2, %3" : "=v"(r) : "v"(s), "s"(cs), "v"(nm)); return r; }
DI void vpkadd(f32x2& a, f32x2 z) { asm volatile("v_pk_add_f32 %0, %0, %1" : "+v"(a) : "v"(z)); }
DI void vexp2(f32x2& y, int h) { float z = y[h]; asm volatile("v_exp_f32_e32 %0, %0" : "+v"(z)); y[h] = z; }
DI float vfma(float s, float nm) { float r; asm volatile("v_fma_f32 %0, %1, %2, %3" : "=v"(r) : "v"(s), "s"(CS), "v"(nm)); return r; }
DI void vexp(float& z) { asm volatile("v_exp_f32_e32 %0, %0" : "+v"(z)); }
DI void vadd(float& a, float z) { asm volatile("v_add_f32_e32 %0, %0, %1" : "+v"(a) : "v"(z)); }
DI float vaddr(float a, float b) { float r; asm volatile("v_add_f32_e32 %0, %1, %2" : "=v"(r) : "v"(a), "v"(b)); return r; }
DI float vrcp(float a) { float r; asm volatile("v_rcp_f32_e32 %0, %1" : "=v"(r) : "v"(a)); return r; }
DI float vmul(float a, float b) { float r; asm volatile("v_mul_f32_e32 %0, %1, %2" : "=v"(r) : "v"(a), "v"(b)); return r; }
DI u32 vcvt(float a, float b) { u32 r; asm volatile("v_cvt_pk_bf16_f32 %0, %1, %2" : "=v"(r) : "v"(a), "v"(b)); return r; }

#define RWEXPR (b + kvh)
DI u32 xsoff(u32 pc, u32 rw) { const u32 sp = (pc * 205u) >> 11, rg = pc - sp * 10; u32 sg = sp + rw; sg = sg >= 7 ? sg - 7 : sg; return rg * 7168 + sg * 128; }
#define XSOFF(p, w) xsoff((p) + (w), rw)
DI u32 wsoff(u32 s, u32 rw) { u32 sg = (s >> 1) + rw; sg = sg >= 7 ? sg - 7 : sg; return (sg * 2 + (s & 1)) * (32 * 8192); }
#define WSOFF(s) wsoff(s, rw)
#define OSOFF(k) ((((k) + ro) & 7) * (32 * 896))

#define SETUP2() do { \
  _Pragma("unroll") for (int j = 0; j < 4; ++j) { \
    aW[j] = rego + g * 1024 + qp * 128 + (((2 * j + (p >> 1)) ^ ((qp & 2) | ((g & 1) << 2))) << 4) + (p & 1) * 8; \
    aK[j] = rego + q15 * 256 + (((4 * j + g) ^ q15) << 4); \
    const u32 rr = 4 * (g & 1) + qp; \
    aV[j] = rego + (g >> 1) * 1024 + rr * 128 + (((2 * j + (p >> 1)) ^ (rr & 6)) << 4) + (p & 1) * 8; \
    vK[j] = (4 * j + g) * 512 + ((q15 ^ (4 * j + g)) << 4) - j * 1024; \
    vV[j] = (8 * j + r8) * 512 + ((c8 ^ (r8 & 6)) << 4) - j * 1024; \
  } \
  _Pragma("unroll") for (int i = 0; i < 2; ++i) aX[i] = (q15 >> 3) * 1024 + (q15 & 7) * 128 + (((4 * i + g) ^ ((q15 >> 1) & 7)) << 4); \
} while (0)

#define SETUP3() do { \
  asm volatile("v_mbcnt_lo_u32_b32 %0, -1, 0\n\tv_mbcnt_hi_u32_b32 %0, -1, %0" : "=v"(lt)); \
  _Pragma("unroll") for (int j = 0; j < 4; ++j) vA[j] = (8 * j + (lt >> 3)) * 896 + (((lt & 7) ^ ((lt >> 3) & 6)) << 4) - j * 1024; \
  aOw[0] = ((wave * 2 - ro) & 7) * 5120 + lt * 16; aOw[1] = ((wave * 2 + 1 - ro) & 7) * 5120 + lt * 16; \
  pb = h * PART_STRIDE + wave * 1024 + lt * 16; pbh = h * PART_STRIDE + 69632 + wave * 512 + lt * 8; \
} while (0)
#define SWAP16(q) do { auto r_ = __builtin_amdgcn_permlane16_swap(__float_as_uint(mx[q]), __float_as_uint(mx[q]), false, false); t0_[q] = __uint_as_float(r_[0]); t1_[q] = __uint_as_float(r_[1]); \
  asm volatile("" : "+v"(t0_[q]), "+v"(t1_[q])); } while (0)
#define SWAP32(q) do { auto r_ = __builtin_amdgcn_permlane32_swap(__float_as_uint(mx[q]), __float_as_uint(mx[q]), false, false); t0_[q] = __uint_as_float(r_[0]); t1_[q] = __uint_as_float(r_[1]); \
  asm volatile("" : "+v"(t0_[q]), "+v"(t1_[q])); } while (0)
#define SM_PREP() do { \
  _Pragma("unroll") for (int q = 0; q < 5; ++q) { nm2[q] = f32x2{nmB[q], nmB[q]}; lp[q] = f32x2{ls2[q][0], ls2[q][1]} * fa[q]; \
    _Pragma("unroll") for (int j = 0; j < 4; ++j) { acc[j][q] *= fa[q]; asm volatile("" : "+v"(acc[j][q])); } } \
} while (0)
#define LOHI(v, h) __builtin_shufflevector(v, v, 2 * (h), 2 * (h) + 1)
#define CODE_TOUCH2() asm volatile("global_load_dword %0, %1, %2" : "+v"(touch2) : "v"(cto), "s"(pc_) : "memory")
#define CODE_TOUCH() do { u32 len_; \
  asm volatile("s_getpc_b64 %0\ngqa_lbl_a:\n\ts_mov_b32 %1, gqa_lbl_z-gqa_lbl_a" : "=s"(pc_), "=s"(len_)); \
  const u32 o_ = (((bx >> 3) * 4 + wave) * 4 + (lane & 3)) * 128; \
  cto = o_ < len_ ? o_ : 0u; CODE_TOUCH2(); } while (0)
#define PAGE_TOUCH() do { \
  const u32 li_ = lane & 31; \
  const char* p_ = li_ < 4 ? (const char*)(kc + (size_t)(b * HKV + kvh) * SK * DH) + li_ * 65536 \
                 : li_ < 8 ? (const char*)(vc + (size_t)(b * HKV + kvh) * SK * DH) + (li_ - 4) * 65536 \
                 : li_ < 12 ? (const char*)(wo + (size_t)h * 256 * DM) + (li_ - 8) * 57344 \
                 : li_ < 28 ? (const char*)(ws + WS_PART + (size_t)(b * HQ + li_ - 12) * PART_STRIDE)        \
                 : (const char*)(flags + 100 * 16);                                                         \
  asm volatile("global_load_ubyte %0, %1, off" : "+v"(touch3) : "v"(p_) : "memory"); } while (0)
#define WO_TOUCH() asm volatile("s_mov_b32 m0, %0\n\ts_nop 0\n\tbuffer_load_dword %1, %2, %3 offen lds" :: "s"(ldsb + 40960 + 3 * 30720 + 28672 + wave * 256), "v"((lt < 28 ? lt : 0u) * 128u), "s"(rO), "s"((b * 4 + wave) * (28 * 128)) : "memory")
#define EPOCH_LOAD() asm volatile("global_load_dwordx2 %0, %1, %2 sc1" : "=v"(e0v) : "v"(0u), "s"(epoch) : "memory")
#define SETUP_B(p0, p1, p2, p3, p4, p5) do { \
  _Pragma("unroll") for (int t = 0; t < 3; ++t) { const u32 by = 96 * wave + 32 * t, ln = by >> 7, cp = (by >> 5) & 3, rr = 4 * ((lt >> 4) & 1) + ((lt >> 2) & 3); \
    const u32 lp = 40960 + (lt >> 5) * 1024 + rr * 128 + (((2 * cp + ((lt >> 1) & 1)) ^ (rr & 6)) << 4) + (lt & 1) * 8; \
    aO[t][0] = lp + (ln == 0 ? p0 : ln == 1 ? p1 : p2); aO[t][1] = lp + (ln == 0 ? p3 : ln == 1 ? p4 : p5); } \
} while (0)
#define PUBLISH(k0, n) __hip_atomic_store(flags + ((k0 + (lt < n ? lt : 0u)) * 4 + wave) * 16 + h, token, __ATOMIC_RELAXED, __HIP_MEMORY_SCOPE_AGENT)
#define BLK0 (wave * 16 + h)
#define PSLEEP "2"
#define POLL() GAS u64* const fp0 = flags + (BLK0) * 16 + (lt & 15); asm volatile("global_load_dwordx2 %0, %1, off sc1" : "+v"(fl) : "v"(fp0) : "memory")
#define PL(r, fp) asm volatile("global_load_dwordx2 %0, %1, off sc1\n\ts_sleep " PSLEEP : "+v"(r) : "v"(fp) : "memory")
#define CK(r, fp) asm volatile("s_waitcnt vmcnt(3)" : "+v"(r) :: "memory"); if (__all(r == token)) break; PL(r, fp)
#define POLL_START(fp) do { PL(fl, fp); PL(f1, fp); PL(f2, fp); PL(f3, fp); } while (0)
#define POLL_WAIT(fp) do { \
    for (u32 spins = 0; spins < (1u << 16); ++spins) { CK(fl, fp); CK(f1, fp); CK(f2, fp); CK(f3, fp); } } while (0)
#define GATHER_REDUCE(blk, e, MID, NMID) do { u32x4 gv[16]; f32x2 a_[4]; \
    _Pragma("unroll") for (int k = 0; k < 16; ++k) \
      asm volatile("buffer_load_dwordx4 %0, %1, %2, %3 offen sc1" : "=v"(gv[k]) : "v"((blk) * 1024 + lt * 16), "s"(rP), "s"(k * PART_STRIDE) : "memory"); \
    asm volatile("s_waitcnt vmcnt(16)" : "+v"(fl), "+v"(f1), "+v"(f2), "+v"(f3) :: "memory"); \
    MID; \
    _Pragma("unroll") for (int k = 0; k < 16; k += 4) { \
      asm volatile("s_waitcnt vmcnt(%4)" : "+v"(gv[k]), "+v"(gv[k + 1]), "+v"(gv[k + 2]), "+v"(gv[k + 3]) : "i"(NMID + 12 - k) : "memory"); \
      _Pragma("unroll") for (int kk = k; kk < k + 4; ++kk) _Pragma("unroll") for (int i = 0; i < 4; ++i) { \
        const f32x2 x_ = {__uint_as_float(gv[kk][i] << 16), __uint_as_float(gv[kk][i] & 0xffff0000u)}; a_[i] = kk == 0 ? x_ : a_[i] + x_; } \
      asm volatile("" : "+v"(a_[0]), "+v"(a_[1]), "+v"(a_[2]), "+v"(a_[3])); } \
    *(u32x2*)(ob + oofs[e][0]) = u32x2{cvtpk(a_[0][0], a_[0][1]), cvtpk(a_[1][0], a_[1][1])}; \
    *(u32x2*)(ob + oofs[e][1]) = u32x2{cvtpk(a_[2][0], a_[2][1]), cvtpk(a_[3][0], a_[3][1])}; } while (0)
#define OUT_ADDR() do { \
  _Pragma("unroll") for (int e = 0; e < 2; ++e) { \
    const u32 blk = e == 0 ? BLK0 : 64 + h; \
    const u32 cw = blk < 68 ? blk & 3 : (blk - 68) * 2 + (lt >> 5); \
    _Pragma("unroll") for (int t = 0; t < 2; ++t) { \
      const u32 tt = blk < 68 ? (blk >> 2) * 2 + t : 34, ln = blk < 68 ? lt : (lt & 31) * 2 + t; \
      const u32 jj = (tt * 205u) >> 10, qq = tt - jj * 5; \
      oofs[e][t] = ((b * SQ + qq * 16 + (ln & 15)) * DM + (jj < 4 ? cw * 64 + jj * 16 : 192 + cw * 48 + jj * 16) + (ln >> 4) * 4) * 2; \
      asm volatile("" : "+v"(oofs[e][t])); } } \
} while (0)

__global__ __launch_bounds__(256, 1) void gqa_fused(const u16* __restrict__ x, const u16* __restrict__ kc, const u16* __restrict__ vc,
                                                    const u16* __restrict__ wq, const u16* __restrict__ wo, u16* __restrict__ out, char* __restrict__ ws) {
  extern __shared__ __attribute__((aligned(16))) char smem[];
  LAS char* const lds = (LAS char*)smem;
  const u32 ldsb = (u32)(size_t)lds;
  const u32 tid = threadIdx.x, lane = tid & 63, wave = __builtin_amdgcn_readfirstlane(tid >> 6);
  const u32 bx = blockIdx.x, kvh = bx & 7, par = (bx >> 3) & 1, b = bx >> 4, h = kvh * 2 + par;
  const u32 q15 = lane & 15, g = lane >> 4, qp = (lane >> 2) & 3, p = lane & 3, r8 = lane >> 3, c8 = lane & 7;
  const u32 rego = 40960 + wave * 30720, regb = ldsb + rego;
  const u32 rw0 = RWEXPR, rw = rw0 - ((rw0 * 37u) >> 8) * 7u, ro = b & 7, wv7 = wave & 1;
  const __amdgpu_buffer_rsrc_t rX = __builtin_amdgcn_make_buffer_rsrc((void*)(x + (size_t)b * SQ * DM), 0, SQ * DM * 2, 0x00020000);
  const __amdgpu_buffer_rsrc_t rW = __builtin_amdgcn_make_buffer_rsrc((void*)(wq + h * 256 + wave * 64), 0, (DM - 1) * 8192 + 128, 0x00020000);
  const __amdgpu_buffer_rsrc_t rK = __builtin_amdgcn_make_buffer_rsrc((void*)(kc + ((size_t)(b * HKV + kvh) * SK + wave * 128) * DH), 0, 128 * 512, 0x00020000);
  const __amdgpu_buffer_rsrc_t rV = __builtin_amdgcn_make_buffer_rsrc((void*)(vc + (size_t)(b * HKV + kvh) * SK * DH + wave * 64), 0, (SK - 1) * 512 + 128, 0x00020000);
  const __amdgpu_buffer_rsrc_t rO = __builtin_amdgcn_make_buffer_rsrc((void*)(wo + (size_t)h * 256 * DM), 0, 256 * 896, 0x00020000);
  const __amdgpu_buffer_rsrc_t rP = __builtin_amdgcn_make_buffer_rsrc((void*)(ws + WS_PART + 128 + (size_t)b * HQ * PART_STRIDE), 0, HQ * PART_STRIDE - 128, 0x00020000);
  GAS u64* const flags = (GAS u64*)(ws + WS_FLAG + b * 16384);
  GAS u64* const epoch = (GAS u64*)(ws + WS_EPOCH + b * 256);
  const u32 vX = r8 * 896 + ((c8 ^ ((4 * (wave & 1) + (lane >> 4)) & 7)) << 4);
  u32 vW[4], vK[4], vV[4], vA[4], aW[4], aK[4], aV[4], aX[2], aO[3][2], touch = 0, touch2 = 0, touch3 = 0, cto;
  u64 pc_;
#pragma unroll
  for (int i = 0; i < 4; ++i) vW[i] = (8 * i + r8) * 8192 + ((c8 ^ ((r8 & 2) | ((i & 1) << 2))) << 4) - i * 1024;
  const u32 aB = lane * 16, aP = lane * 16 + wave * 5120, aQ = q15 * 16 + (g >> 1) * 256 + (g & 1) * 8 + wave * 10240;
  u32 aOw[2], pb, pbh, lt;
  bf16x8 A[2][4], B[2][5], B2[3][5], F[8][5], Wf[14][4], Kf[16][4], Vf[16][4];
  float y[3][8], ls2[5][2], nmA[5], nmB[5], mA[5], fa[5];
  f32x4 mm[5];
  f32x2 yp[3][4], nm2[5], lp[5];
  u32 P2[5][4], P3[5][4];
  const float ncs = -CS;
  const u32 aSr = q15 * 16, aS = aSr + wave * 4;
  u32 pq[3][4];
  f32x4 acc[7][5], Sc[8][5];
  float mx[5], ls[5], rl[5], z[8], t0_[5], t1_[5];
  u32 pk[4], oofs[2][2];
  u64 fl = 0;
  u64 e0v;
  asm volatile("s_nop 4" ::: "memory");
  dma<0,false,true>(ldsb + wave * 1024 + 0, vX, rX, XSOFF(0, wave));
  dma<0,false,true>(ldsb + wave * 1024 + 4096, vX, rX, XSOFF(4, wave));
  dma<0,false,true>(ldsb + wave * 1024 + 8192, vX, rX, XSOFF(8, wave));
  dma<0,false,true>(ldsb + wave * 1024 + 12288, vX, rX, XSOFF(12, wave));
  dma<0,false,true>(ldsb + wave * 1024 + 16384, vX, rX, XSOFF(16, wave));
  dma<0,false,true>(regb + 0, vW[0], rW, WSOFF(0));
  dma<1024,false,false>(regb + 0, vW[1], rW, WSOFF(0));
  dma<2048,false,false>(regb + 0, vW[2], rW, WSOFF(0));
  dma<3072,false,false>(regb + 0, vW[3], rW, WSOFF(0));
  CODE_TOUCH();
  EPOCH_LOAD();
  PAGE_TOUCH();
  SETUP2();
  dma<0,false,true>(regb + 4096, vW[0], rW, WSOFF(1));
  dma<1024,false,false>(regb + 4096, vW[1], rW, WSOFF(1));
  dma<2048,false,false>(regb + 4096, vW[2], rW, WSOFF(1));
  dma<3072,false,false>(regb + 4096, vW[3], rW, WSOFF(1));
  wait_vm<7>();
  barrier();
  B[0][0] = ld128(lds, aX[0] + 0);
  B[0][1] = ld128(lds, aX[0] + 2048);
  B[0][2] = ld128(lds, aX[0] + 4096);
  B[0][3] = ld128(lds, aX[0] + 6144);
  B[0][4] = ld128(lds, aX[0] + 8192);
  Wf[0][0] = ldtr(lds, aW[0] + 0, aW[0] + 512);
  Wf[0][1] = ldtr(lds, aW[1] + 0, aW[1] + 512);
  Wf[0][2] = ldtr(lds, aW[2] + 0, aW[2] + 512);
  Wf[0][3] = ldtr(lds, aW[3] + 0, aW[3] + 512);
  dma<0,false,true>(ldsb + wave * 1024 + 20480, vX, rX, XSOFF(20, wave));
  dma<0,false,true>(ldsb + wave * 1024 + 24576, vX, rX, XSOFF(24, wave));
  dma<0,false,true>(ldsb + wave * 1024 + 28672, vX, rX, XSOFF(28, wave));
  dma<0,false,true>(ldsb + wave * 1024 + 32768, vX, rX, XSOFF(32, wave));
  wait_lgkm0();
  mfmab<true>(acc[0][0], Wf[0][0], B[0][0]);
  dma<0,false,true>(ldsb + wave * 1024 + 36864, vX, rX, XSOFF(36, wave));
  dma<0,false,true>(regb + 0, vW[0], rW, WSOFF(2));
  mfmab<true>(acc[0][1], Wf[0][0], B[0][1]);
  B[1][0] = ld128(lds, aX[1] + 0);
  B[1][1] = ld128(lds, aX[1] + 2048);
  dma<1024,false,false>(regb + 0, vW[1], rW, WSOFF(2));
  mfmab<true>(acc[0][2], Wf[0][0], B[0][2]);
  B[1][2] = ld128(lds, aX[1] + 4096);
  mfmab<true>(acc[0][3], Wf[0][0], B[0][3]);
  B[1][3] = ld128(lds, aX[1] + 6144);
  B[1][4] = ld128(lds, aX[1] + 8192);
  mfmab<true>(acc[0][4], Wf[0][0], B[0][4]);
  wait_vm<7>();
  Wf[1][0] = ldtr(lds, aW[0] + 4096, aW[0] + 4608);
  dma<2048,false,false>(regb + 0, vW[2], rW, WSOFF(2));
  mfmab<true>(acc[1][0], Wf[0][1], B[0][0]);
  Wf[1][1] = ldtr(lds, aW[1] + 4096, aW[1] + 4608);
  dma<3072,false,false>(regb + 0, vW[3], rW, WSOFF(2));
  mfmab<true>(acc[1][1], Wf[0][1], B[0][1]);
  dma<0,false,true>(regb + 8192, vW[0], rW, WSOFF(3));
  dma<1024,false,false>(regb + 8192, vW[1], rW, WSOFF(3));
  mfmab<true>(acc[1][2], Wf[0][1], B[0][2]);
  Wf[1][2] = ldtr(lds, aW[2] + 4096, aW[2] + 4608);
  mfmab<true>(acc[1][3], Wf[0][1], B[0][3]);
  Wf[1][3] = ldtr(lds, aW[3] + 4096, aW[3] + 4608);
  mfmab<true>(acc[1][4], Wf[0][1], B[0][4]);
  mfmab<true>(acc[2][0], Wf[0][2], B[0][0]);
  mfmab<true>(acc[2][1], Wf[0][2], B[0][1]);
  mfmab<true>(acc[2][2], Wf[0][2], B[0][2]);
  dma<2048,false,false>(regb + 8192, vW[2], rW, WSOFF(3));
  mfmab<true>(acc[2][3], Wf[0][2], B[0][3]);
  mfmab<true>(acc[2][4], Wf[0][2], B[0][4]);
  mfmab<true>(acc[3][0], Wf[0][3], B[0][0]);
  mfmab<true>(acc[3][1], Wf[0][3], B[0][1]);
  mfmab<true>(acc[3][2], Wf[0][3], B[0][2]);
  dma<3072,false,false>(regb + 8192, vW[3], rW, WSOFF(3));
  mfmab<true>(acc[3][3], Wf[0][3], B[0][3]);
  mfmab<true>(acc[3][4], Wf[0][3], B[0][4]);
  wait_lgkm0();
  asm volatile("" : "+v"(e0v), "+v"(touch), "+v"(touch2), "+v"(touch3));
  const u64 e0 = ((u64)(u32)__builtin_amdgcn_readfirstlane((u32)(e0v >> 32)) << 32) | (u32)__builtin_amdgcn_readfirstlane((u32)e0v);
  const u64 token = (e0 ^ (e0 >> 29)) * 0x9E3779B97F4A7C15ull + 0x632BE59BD9B4E019ull;
  mfmab<false>(acc[0][0], Wf[1][0], B[1][0]);
  mfmab<false>(acc[0][1], Wf[1][0], B[1][1]);
  B[0][0] = ld128(lds, aX[0] + 10240);
  B[0][1] = ld128(lds, aX[0] + 12288);
  mfmab<false>(acc[0][2], Wf[1][0], B[1][2]);
  B[0][2] = ld128(lds, aX[0] + 14336);
  dma<0,false,true>(ldsb + wave * 1024 + 61440, vX, rX, XSOFF(40, wave));
  mfmab<false>(acc[0][3], Wf[1][0], B[1][3]);
  B[0][3] = ld128(lds, aX[0] + 16384);
  B[0][4] = ld128(lds, aX[0] + 18432);
  mfmab<false>(acc[0][4], Wf[1][0], B[1][4]);
  wait_vm<5>();
  Wf[2][0] = ldtr(lds, aW[0] + 0, aW[0] + 512);
  dma<0,false,true>(ldsb + wave * 1024 + 65536, vX, rX, XSOFF(44, wave));
  mfmab<false>(acc[1][0], Wf[1][1], B[1][0]);
  Wf[2][1] = ldtr(lds, aW[1] + 0, aW[1] + 512);
  dma<0,false,true>(ldsb + wave * 1024 + 92160, vX, rX, XSOFF(48, wave));
  mfmab<false>(acc[1][1], Wf[1][1], B[1][1]);
  dma<0,false,true>(ldsb + wave * 1024 + 96256, vX, rX, XSOFF(52, wave));
  dma<0,false,true>(ldsb + wave * 1024 + 122880, vX, rX, XSOFF(56, wave));
  mfmab<false>(acc[1][2], Wf[1][1], B[1][2]);
  Wf[2][2] = ldtr(lds, aW[2] + 0, aW[2] + 512);
  dma<0,false,true>(ldsb + wave * 1024 + 126976, vX, rX, XSOFF(60, wave));
  mfmab<false>(acc[1][3], Wf[1][1], B[1][3]);
  Wf[2][3] = ldtr(lds, aW[3] + 0, aW[3] + 512);
  dma<0,false,true>(ldsb + wave * 1024 + 153600, vX, rX, XSOFF(64, wave));
  mfmab<false>(acc[1][4], Wf[1][1], B[1][4]);
  dma<0,false,true>(ldsb + wv7 * 1024 + 157696, vX, rX, XSOFF(68, wv7));
  dma<0,false,true>(regb + 4096, vW[0], rW, WSOFF(4));
  mfmab<false>(acc[2][0], Wf[1][2], B[1][0]);
  mfmab<false>(acc[2][1], Wf[1][2], B[1][1]);
  dma<1024,false,false>(regb + 4096, vW[1], rW, WSOFF(4));
  mfmab<false>(acc[2][2], Wf[1][2], B[1][2]);
  mfmab<false>(acc[2][3], Wf[1][2], B[1][3]);
  mfmab<false>(acc[2][4], Wf[1][2], B[1][4]);
  dma<2048,false,false>(regb + 4096, vW[2], rW, WSOFF(4));
  mfmab<false>(acc[3][0], Wf[1][3], B[1][0]);
  mfmab<false>(acc[3][1], Wf[1][3], B[1][1]);
  dma<3072,false,false>(regb + 4096, vW[3], rW, WSOFF(4));
  mfmab<false>(acc[3][2], Wf[1][3], B[1][2]);
  mfmab<false>(acc[3][3], Wf[1][3], B[1][3]);
  mfmab<false>(acc[3][4], Wf[1][3], B[1][4]);
  wait_lgkm0();
  mfmab<false>(acc[0][0], Wf[2][0], B[0][0]);
  mfmab<false>(acc[0][1], Wf[2][0], B[0][1]);
  B[1][0] = ld128(lds, aX[1] + 10240);
  B[1][1] = ld128(lds, aX[1] + 12288);
  mfmab<false>(acc[0][2], Wf[2][0], B[0][2]);
  B[1][2] = ld128(lds, aX[1] + 14336);
  mfmab<false>(acc[0][3], Wf[2][0], B[0][3]);
  B[1][3] = ld128(lds, aX[1] + 16384);
  B[1][4] = ld128(lds, aX[1] + 18432);
  mfmab<false>(acc[0][4], Wf[2][0], B[0][4]);
  wait_vm<12>();
  Wf[3][0] = ldtr(lds, aW[0] + 8192, aW[0] + 8704);
  mfmab<false>(acc[1][0], Wf[2][1], B[0][0]);
  Wf[3][1] = ldtr(lds, aW[1] + 8192, aW[1] + 8704);
  mfmab<false>(acc[1][1], Wf[2][1], B[0][1]);
  mfmab<false>(acc[1][2], Wf[2][1], B[0][2]);
  Wf[3][2] = ldtr(lds, aW[2] + 8192, aW[2] + 8704);
  mfmab<false>(acc[1][3], Wf[2][1], B[0][3]);
  Wf[3][3] = ldtr(lds, aW[3] + 8192, aW[3] + 8704);
  dma<0,false,true>(regb + 0, vW[0], rW, WSOFF(5));
  mfmab<false>(acc[1][4], Wf[2][1], B[0][4]);
  mfmab<false>(acc[2][0], Wf[2][2], B[0][0]);
  dma<1024,false,false>(regb + 0, vW[1], rW, WSOFF(5));
  mfmab<false>(acc[2][1], Wf[2][2], B[0][1]);
  mfmab<false>(acc[2][2], Wf[2][2], B[0][2]);
  mfmab<false>(acc[2][3], Wf[2][2], B[0][3]);
  dma<2048,false,false>(regb + 0, vW[2], rW, WSOFF(5));
  mfmab<false>(acc[2][4], Wf[2][2], B[0][4]);
  mfmab<false>(acc[3][0], Wf[2][3], B[0][0]);
  dma<3072,false,false>(regb + 0, vW[3], rW, WSOFF(5));
  mfmab<false>(acc[3][1], Wf[2][3], B[0][1]);
  mfmab<false>(acc[3][2], Wf[2][3], B[0][2]);
  mfmab<false>(acc[3][3], Wf[2][3], B[0][3]);
  dma<0,false,true>(regb + 12288, vW[0], rW, WSOFF(6));
  mfmab<false>(acc[3][4], Wf[2][3], B[0][4]);
  barrier();
  mfmab<false>(acc[0][0], Wf[3][0], B[1][0]);
  dma<1024,false,false>(regb + 12288, vW[1], rW, WSOFF(6));
  mfmab<false>(acc[0][1], Wf[3][0], B[1][1]);
  B[0][0] = ld128(lds, aX[0] + 20480);
  B[0][1] = ld128(lds, aX[0] + 22528);
  dma<2048,false,false>(regb + 12288, vW[2], rW, WSOFF(6));
  mfmab<false>(acc[0][2], Wf[3][0], B[1][2]);
  B[0][2] = ld128(lds, aX[0] + 24576);
  mfmab<false>(acc[0][3], Wf[3][0], B[1][3]);
  B[0][3] = ld128(lds, aX[0] + 26624);
  B[0][4] = ld128(lds, aX[0] + 28672);
  mfmab<false>(acc[0][4], Wf[3][0], B[1][4]);
  wait_vm<7>();
  Wf[4][0] = ldtr(lds, aW[0] + 4096, aW[0] + 4608);
  dma<3072,false,false>(regb + 12288, vW[3], rW, WSOFF(6));
  mfmab<false>(acc[1][0], Wf[3][1], B[1][0]);
  Wf[4][1] = ldtr(lds, aW[1] + 4096, aW[1] + 4608);
  dma<0,false,true>(regb + 8192, vW[0], rW, WSOFF(7));
  mfmab<false>(acc[1][1], Wf[3][1], B[1][1]);
  dma<1024,false,false>(regb + 8192, vW[1], rW, WSOFF(7));
  dma<2048,false,false>(regb + 8192, vW[2], rW, WSOFF(7));
  mfmab<false>(acc[1][2], Wf[3][1], B[1][2]);
  Wf[4][2] = ldtr(lds, aW[2] + 4096, aW[2] + 4608);
  dma<3072,false,false>(regb + 8192, vW[3], rW, WSOFF(7));
  mfmab<false>(acc[1][3], Wf[3][1], B[1][3]);
  Wf[4][3] = ldtr(lds, aW[3] + 4096, aW[3] + 4608);
  mfmab<false>(acc[1][4], Wf[3][1], B[1][4]);
  dma<0,false,true>(regb + 16384, vW[0], rW, WSOFF(8));
  mfmab<false>(acc[2][0], Wf[3][2], B[1][0]);
  mfmab<false>(acc[2][1], Wf[3][2], B[1][1]);
  mfmab<false>(acc[2][2], Wf[3][2], B[1][2]);
  dma<1024,false,false>(regb + 16384, vW[1], rW, WSOFF(8));
  mfmab<false>(acc[2][3], Wf[3][2], B[1][3]);
  mfmab<false>(acc[2][4], Wf[3][2], B[1][4]);
  dma<2048,false,false>(regb + 16384, vW[2], rW, WSOFF(8));
  mfmab<false>(acc[3][0], Wf[3][3], B[1][0]);
  mfmab<false>(acc[3][1], Wf[3][3], B[1][1]);
  mfmab<false>(acc[3][2], Wf[3][3], B[1][2]);
  dma<3072,false,false>(regb + 16384, vW[3], rW, WSOFF(8));
  mfmab<false>(acc[3][3], Wf[3][3], B[1][3]);
  mfmab<false>(acc[3][4], Wf[3][3], B[1][4]);
  wait_lgkm0();
  mfmab<false>(acc[0][0], Wf[4][0], B[0][0]);
  dma<0,false,true>(regb + 4096, vW[0], rW, WSOFF(9));
  mfmab<false>(acc[0][1], Wf[4][0], B[0][1]);
  B[1][0] = ld128(lds, aX[1] + 20480);
  B[1][1] = ld128(lds, aX[1] + 22528);
  mfmab<false>(acc[0][2], Wf[4][0], B[0][2]);
  B[1][2] = ld128(lds, aX[1] + 24576);
  dma<1024,false,false>(regb + 4096, vW[1], rW, WSOFF(9));
  mfmab<false>(acc[0][3], Wf[4][0], B[0][3]);
  B[1][3] = ld128(lds, aX[1] + 26624);
  B[1][4] = ld128(lds, aX[1] + 28672);
  mfmab<false>(acc[0][4], Wf[4][0], B[0][4]);
  wait_vm<14>();
  Wf[5][0] = ldtr(lds, aW[0] + 0, aW[0] + 512);
  mfmab<false>(acc[1][0], Wf[4][1], B[0][0]);
  Wf[5][1] = ldtr(lds, aW[1] + 0, aW[1] + 512);
  mfmab<false>(acc[1][1], Wf[4][1], B[0][1]);
  mfmab<false>(acc[1][2], Wf[4][1], B[0][2]);
  Wf[5][2] = ldtr(lds, aW[2] + 0, aW[2] + 512);
  mfmab<false>(acc[1][3], Wf[4][1], B[0][3]);
  Wf[5][3] = ldtr(lds, aW[3] + 0, aW[3] + 512);
  dma<2048,false,false>(regb + 4096, vW[2], rW, WSOFF(9));
  mfmab<false>(acc[1][4], Wf[4][1], B[0][4]);
  mfmab<false>(acc[2][0], Wf[4][2], B[0][0]);
  dma<3072,false,false>(regb + 4096, vW[3], rW, WSOFF(9));
  mfmab<false>(acc[2][1], Wf[4][2], B[0][1]);
  mfmab<false>(acc[2][2], Wf[4][2], B[0][2]);
  mfmab<false>(acc[2][3], Wf[4][2], B[0][3]);
  mfmab<false>(acc[2][4], Wf[4][2], B[0][4]);
  mfmab<false>(acc[3][0], Wf[4][3], B[0][0]);
  mfmab<false>(acc[3][1], Wf[4][3], B[0][1]);
  mfmab<false>(acc[3][2], Wf[4][3], B[0][2]);
  mfmab<false>(acc[3][3], Wf[4][3], B[0][3]);
  mfmab<false>(acc[3][4], Wf[4][3], B[0][4]);
  wait_lgkm0();
  mfmab<false>(acc[0][0], Wf[5][0], B[1][0]);
  dma<0,false,true>(regb + 0, vW[0], rW, WSOFF(10));
  dma<1024,false,false>(regb + 0, vW[1], rW, WSOFF(10));
  mfmab<false>(acc[0][1], Wf[5][0], B[1][1]);
  B[0][0] = ld128(lds, aX[0] + 30720);
  B[0][1] = ld128(lds, aX[0] + 32768);
  dma<2048,false,false>(regb + 0, vW[2], rW, WSOFF(10));
  mfmab<false>(acc[0][2], Wf[5][0], B[1][2]);
  B[0][2] = ld128(lds, aX[0] + 34816);
  B[0][3] = ld128(lds, aX[0] + 36864);
  dma<3072,false,false>(regb + 0, vW[3], rW, WSOFF(10));
  mfmab<false>(acc[0][3], Wf[5][0], B[1][3]);
  B[0][4] = ld128(lds, aX[0] + 38912);
  mfmab<false>(acc[0][4], Wf[5][0], B[1][4]);
  wait_vm<16>();
  Wf[6][0] = ldtr(lds, aW[0] + 12288, aW[0] + 12800);
  mfmab<false>(acc[1][0], Wf[5][1], B[1][0]);
  Wf[6][1] = ldtr(lds, aW[1] + 12288, aW[1] + 12800);
  mfmab<false>(acc[1][1], Wf[5][1], B[1][1]);
  Wf[6][2] = ldtr(lds, aW[2] + 12288, aW[2] + 12800);
  mfmab<false>(acc[1][2], Wf[5][1], B[1][2]);
  Wf[6][3] = ldtr(lds, aW[3] + 12288, aW[3] + 12800);
  mfmab<false>(acc[1][3], Wf[5][1], B[1][3]);
  mfmab<false>(acc[1][4], Wf[5][1], B[1][4]);
  wait_vm<12>();
  Wf[7][0] = ldtr(lds, aW[0] + 8192, aW[0] + 8704);
  mfmab<false>(acc[2][0], Wf[5][2], B[1][0]);
  Wf[7][1] = ldtr(lds, aW[1] + 8192, aW[1] + 8704);
  mfmab<false>(acc[2][1], Wf[5][2], B[1][1]);
  Wf[7][2] = ldtr(lds, aW[2] + 8192, aW[2] + 8704);
  mfmab<false>(acc[2][2], Wf[5][2], B[1][2]);
  Wf[7][3] = ldtr(lds, aW[3] + 8192, aW[3] + 8704);
  mfmab<false>(acc[2][3], Wf[5][2], B[1][3]);
  mfmab<false>(acc[2][4], Wf[5][2], B[1][4]);
  mfmab<false>(acc[3][0], Wf[5][3], B[1][0]);
  mfmab<false>(acc[3][1], Wf[5][3], B[1][1]);
  mfmab<false>(acc[3][2], Wf[5][3], B[1][2]);
  mfmab<false>(acc[3][3], Wf[5][3], B[1][3]);
  mfmab<false>(acc[3][4], Wf[5][3], B[1][4]);
  wait_lgkm0();
  mfmab<false>(acc[0][0], Wf[6][0], B[0][0]);
  dma<0,false,true>(regb + 8192, vW[0], rW, WSOFF(11));
  dma<1024,false,false>(regb + 8192, vW[1], rW, WSOFF(11));
  mfmab<false>(acc[0][1], Wf[6][0], B[0][1]);
  B[1][0] = ld128(lds, aX[1] + 30720);
  B[1][1] = ld128(lds, aX[1] + 32768);
  dma<2048,false,false>(regb + 8192, vW[2], rW, WSOFF(11));
  mfmab<false>(acc[0][2], Wf[6][0], B[0][2]);
  B[1][2] = ld128(lds, aX[1] + 34816);
  B[1][3] = ld128(lds, aX[1] + 36864);
  dma<3072,false,false>(regb + 8192, vW[3], rW, WSOFF(11));
  mfmab<false>(acc[0][3], Wf[6][0], B[0][3]);
  B[1][4] = ld128(lds, aX[1] + 38912);
  dma<0,false,true>(regb + 12288, vW[0], rW, WSOFF(12));
  dma<1024,false,false>(regb + 12288, vW[1], rW, WSOFF(12));
  mfmab<false>(acc[0][4], Wf[6][0], B[0][4]);
  wait_vm<14>();
  Wf[8][0] = ldtr(lds, aW[0] + 16384, aW[0] + 16896);
  dma<2048,false,false>(regb + 12288, vW[2], rW, WSOFF(12));
  mfmab<false>(acc[1][0], Wf[6][1], B[0][0]);
  Wf[8][1] = ldtr(lds, aW[1] + 16384, aW[1] + 16896);
  dma<3072,false,false>(regb + 12288, vW[3], rW, WSOFF(12));
  mfmab<false>(acc[1][1], Wf[6][1], B[0][1]);
  Wf[8][2] = ldtr(lds, aW[2] + 16384, aW[2] + 16896);
  mfmab<false>(acc[1][2], Wf[6][1], B[0][2]);
  Wf[8][3] = ldtr(lds, aW[3] + 16384, aW[3] + 16896);
  mfmab<false>(acc[1][3], Wf[6][1], B[0][3]);
  mfmab<false>(acc[1][4], Wf[6][1], B[0][4]);
  wait_vm<12>();
  Wf[9][0] = ldtr(lds, aW[0] + 4096, aW[0] + 4608);
  mfmab<false>(acc[2][0], Wf[6][2], B[0][0]);
  Wf[9][1] = ldtr(lds, aW[1] + 4096, aW[1] + 4608);
  mfmab<false>(acc[2][1], Wf[6][2], B[0][1]);
  Wf[9][2] = ldtr(lds, aW[2] + 4096, aW[2] + 4608);
  mfmab<false>(acc[2][2], Wf[6][2], B[0][2]);
  Wf[9][3] = ldtr(lds, aW[3] + 4096, aW[3] + 4608);
  mfmab<false>(acc[2][3], Wf[6][2], B[0][3]);
  mfmab<false>(acc[2][4], Wf[6][2], B[0][4]);
  mfmab<false>(acc[3][0], Wf[6][3], B[0][0]);
  mfmab<false>(acc[3][1], Wf[6][3], B[0][1]);
  mfmab<false>(acc[3][2], Wf[6][3], B[0][2]);
  mfmab<false>(acc[3][3], Wf[6][3], B[0][3]);
  mfmab<false>(acc[3][4], Wf[6][3], B[0][4]);
  barrier();
  mfmab<false>(acc[0][0], Wf[7][0], B[1][0]);
  dma<0,false,true>(regb + 4096, vW[0], rW, WSOFF(13));
  dma<1024,false,false>(regb + 4096, vW[1], rW, WSOFF(13));
  mfmab<false>(acc[0][1], Wf[7][0], B[1][1]);
  B[0][0] = ld128(lds, aX[0] + 61440);
  B[0][1] = ld128(lds, aX[0] + 63488);
  dma<2048,false,false>(regb + 4096, vW[2], rW, WSOFF(13));
  mfmab<false>(acc[0][2], Wf[7][0], B[1][2]);
  B[0][2] = ld128(lds, aX[0] + 65536);
  dma<3072,false,false>(regb + 4096, vW[3], rW, WSOFF(13));
  mfmab<false>(acc[0][3], Wf[7][0], B[1][3]);
  B[0][3] = ld128(lds, aX[0] + 67584);
  B[0][4] = ld128(lds, aX[0] + 92160);
  mfmab<false>(acc[0][4], Wf[7][0], B[1][4]);
  wait_vm<12>();
  Wf[10][0] = ldtr(lds, aW[0] + 0, aW[0] + 512);
  dma<0,true,true>(regb + 16384, vK[0], rK, 0u);
  mfmab<false>(acc[1][0], Wf[7][1], B[1][0]);
  Wf[10][1] = ldtr(lds, aW[1] + 0, aW[1] + 512);
  mfmab<false>(acc[1][1], Wf[7][1], B[1][1]);
  mfmab<false>(acc[1][2], Wf[7][1], B[1][2]);
  Wf[10][2] = ldtr(lds, aW[2] + 0, aW[2] + 512);
  dma<1024,true,false>(regb + 16384, vK[1], rK, 0u);
  mfmab<false>(acc[1][3], Wf[7][1], B[1][3]);
  Wf[10][3] = ldtr(lds, aW[3] + 0, aW[3] + 512);
  mfmab<false>(acc[1][4], Wf[7][1], B[1][4]);
  dma<2048,true,false>(regb + 16384, vK[2], rK, 0u);
  mfmab<false>(acc[2][0], Wf[7][2], B[1][0]);
  mfmab<false>(acc[2][1], Wf[7][2], B[1][1]);
  dma<3072,true,false>(regb + 16384, vK[3], rK, 0u);
  mfmab<false>(acc[2][2], Wf[7][2], B[1][2]);
  mfmab<false>(acc[2][3], Wf[7][2], B[1][3]);
  mfmab<false>(acc[2][4], Wf[7][2], B[1][4]);
  mfmab<false>(acc[3][0], Wf[7][3], B[1][0]);
  mfmab<false>(acc[3][1], Wf[7][3], B[1][1]);
  mfmab<false>(acc[3][2], Wf[7][3], B[1][2]);
  mfmab<false>(acc[3][3], Wf[7][3], B[1][3]);
  mfmab<false>(acc[3][4], Wf[7][3], B[1][4]);
  wait_lgkm0();
  mfmab<false>(acc[0][0], Wf[8][0], B[0][0]);
  dma<0,true,true>(regb + 0, vK[0], rK, 256u);
  dma<1024,true,false>(regb + 0, vK[1], rK, 256u);
  mfmab<false>(acc[0][1], Wf[8][0], B[0][1]);
  B[1][0] = ld128(lds, aX[1] + 61440);
  B[1][1] = ld128(lds, aX[1] + 63488);
  dma<2048,true,false>(regb + 0, vK[2], rK, 256u);
  mfmab<false>(acc[0][2], Wf[8][0], B[0][2]);
  B[1][2] = ld128(lds, aX[1] + 65536);
  B[1][3] = ld128(lds, aX[1] + 67584);
  mfmab<false>(acc[0][3], Wf[8][0], B[0][3]);
  B[1][4] = ld128(lds, aX[1] + 92160);
  mfmab<false>(acc[0][4], Wf[8][0], B[0][4]);
  wait_vm<15>();
  Wf[11][0] = ldtr(lds, aW[0] + 8192, aW[0] + 8704);
  dma<3072,true,false>(regb + 0, vK[3], rK, 256u);
  mfmab<false>(acc[1][0], Wf[8][1], B[0][0]);
  Wf[11][1] = ldtr(lds, aW[1] + 8192, aW[1] + 8704);
  mfmab<false>(acc[1][1], Wf[8][1], B[0][1]);
  Wf[11][2] = ldtr(lds, aW[2] + 8192, aW[2] + 8704);
  mfmab<false>(acc[1][2], Wf[8][1], B[0][2]);
  Wf[11][3] = ldtr(lds, aW[3] + 8192, aW[3] + 8704);
  mfmab<false>(acc[1][3], Wf[8][1], B[0][3]);
  mfmab<false>(acc[1][4], Wf[8][1], B[0][4]);
  wait_vm<12>();
  Wf[12][0] = ldtr(lds, aW[0] + 12288, aW[0] + 12800);
  mfmab<false>(acc[2][0], Wf[8][2], B[0][0]);
  Wf[12][1] = ldtr(lds, aW[1] + 12288, aW[1] + 12800);
  mfmab<false>(acc[2][1], Wf[8][2], B[0][1]);
  Wf[12][2] = ldtr(lds, aW[2] + 12288, aW[2] + 12800);
  mfmab<false>(acc[2][2], Wf[8][2], B[0][2]);
  Wf[12][3] = ldtr(lds, aW[3] + 12288, aW[3] + 12800);
  mfmab<false>(acc[2][3], Wf[8][2], B[0][3]);
  mfmab<false>(acc[2][4], Wf[8][2], B[0][4]);
  mfmab<false>(acc[3][0], Wf[8][3], B[0][0]);
  mfmab<false>(acc[3][1], Wf[8][3], B[0][1]);
  mfmab<false>(acc[3][2], Wf[8][3], B[0][2]);
  mfmab<false>(acc[3][3], Wf[8][3], B[0][3]);
  mfmab<false>(acc[3][4], Wf[8][3], B[0][4]);
  wait_lgkm0();
  mfmab<false>(acc[0][0], Wf[9][0], B[1][0]);
  dma<0,true,true>(regb + 8192, vK[0], rK, 8192u);
  dma<1024,true,false>(regb + 8192, vK[1], rK, 8192u);
  mfmab<false>(acc[0][1], Wf[9][0], B[1][1]);
  B[0][0] = ld128(lds, aX[0] + 94208);
  B[0][1] = ld128(lds, aX[0] + 96256);
  dma<2048,true,false>(regb + 8192, vK[2], rK, 8192u);
  mfmab<false>(acc[0][2], Wf[9][0], B[1][2]);
  B[0][2] = ld128(lds, aX[0] + 98304);
  mfmab<false>(acc[0][3], Wf[9][0], B[1][3]);
  B[0][3] = ld128(lds, aX[0] + 122880);
  B[0][4] = ld128(lds, aX[0] + 124928);
  mfmab<false>(acc[0][4], Wf[9][0], B[1][4]);
  wait_vm<11>();
  Wf[13][0] = ldtr(lds, aW[0] + 4096, aW[0] + 4608);
  mfmab<false>(acc[1][0], Wf[9][1], B[1][0]);
  Wf[13][1] = ldtr(lds, aW[1] + 4096, aW[1] + 4608);
  mfmab<false>(acc[1][1], Wf[9][1], B[1][1]);
  mfmab<false>(acc[1][2], Wf[9][1], B[1][2]);
  Wf[13][2] = ldtr(lds, aW[2] + 4096, aW[2] + 4608);
  mfmab<false>(acc[1][3], Wf[9][1], B[1][3]);
  Wf[13][3] = ldtr(lds, aW[3] + 4096, aW[3] + 4608);
  mfmab<false>(acc[1][4], Wf[9][1], B[1][4]);
  mfmab<false>(acc[2][0], Wf[9][2], B[1][0]);
  mfmab<false>(acc[2][1], Wf[9][2], B[1][1]);
  mfmab<false>(acc[2][2], Wf[9][2], B[1][2]);
  mfmab<false>(acc[2][3], Wf[9][2], B[1][3]);
  mfmab<false>(acc[2][4], Wf[9][2], B[1][4]);
  mfmab<false>(acc[3][0], Wf[9][3], B[1][0]);
  dma<3072,true,false>(regb + 8192, vK[3], rK, 8192u);
  mfmab<false>(acc[3][1], Wf[9][3], B[1][1]);
  mfmab<false>(acc[3][2], Wf[9][3], B[1][2]);
  mfmab<false>(acc[3][3], Wf[9][3], B[1][3]);
  mfmab<false>(acc[3][4], Wf[9][3], B[1][4]);
  wait_lgkm0();
  mfmab<false>(acc[0][0], Wf[10][0], B[0][0]);
  mfmab<false>(acc[0][1], Wf[10][0], B[0][1]);
  B[1][0] = ld128(lds, aX[1] + 94208);
  dma<0,true,true>(regb + 4096, vK[0], rK, 8448u);
  mfmab<false>(acc[0][2], Wf[10][0], B[0][2]);
  B[1][1] = ld128(lds, aX[1] + 96256);
  mfmab<false>(acc[0][3], Wf[10][0], B[0][3]);
  B[1][2] = ld128(lds, aX[1] + 98304);
  mfmab<false>(acc[0][4], Wf[10][0], B[0][4]);
  B[1][3] = ld128(lds, aX[1] + 122880);
  mfmab<false>(acc[1][0], Wf[10][1], B[0][0]);
  B[1][4] = ld128(lds, aX[1] + 124928);
  mfmab<false>(acc[1][1], Wf[10][1], B[0][1]);
  mfmab<false>(acc[1][2], Wf[10][1], B[0][2]);
  dma<1024,true,false>(regb + 4096, vK[1], rK, 8448u);
  mfmab<false>(acc[1][3], Wf[10][1], B[0][3]);
  mfmab<false>(acc[1][4], Wf[10][1], B[0][4]);
  mfmab<false>(acc[2][0], Wf[10][2], B[0][0]);
  mfmab<false>(acc[2][1], Wf[10][2], B[0][1]);
  mfmab<false>(acc[2][2], Wf[10][2], B[0][2]);
  mfmab<false>(acc[2][3], Wf[10][2], B[0][3]);
  dma<2048,true,false>(regb + 4096, vK[2], rK, 8448u);
  mfmab<false>(acc[2][4], Wf[10][2], B[0][4]);
  mfmab<false>(acc[3][0], Wf[10][3], B[0][0]);
  mfmab<false>(acc[3][1], Wf[10][3], B[0][1]);
  mfmab<false>(acc[3][2], Wf[10][3], B[0][2]);
  mfmab<false>(acc[3][3], Wf[10][3], B[0][3]);
  mfmab<false>(acc[3][4], Wf[10][3], B[0][4]);
  dma<3072,true,false>(regb + 4096, vK[3], rK, 8448u);
  wait_lgkm0();
  mfmab<false>(acc[0][0], Wf[11][0], B[1][0]);
  mfmab<false>(acc[0][1], Wf[11][0], B[1][1]);
  B[0][0] = ld128(lds, aX[0] + 126976);
  mfmab<false>(acc[0][2], Wf[11][0], B[1][2]);
  B[0][1] = ld128(lds, aX[0] + 129024);
  mfmab<false>(acc[0][3], Wf[11][0], B[1][3]);
  B[0][2] = ld128(lds, aX[0] + 153600);
  mfmab<false>(acc[0][4], Wf[11][0], B[1][4]);
  B[0][3] = ld128(lds, aX[0] + 155648);
  mfmab<false>(acc[1][0], Wf[11][1], B[1][0]);
  B[0][4] = ld128(lds, aX[0] + 157696);
  dma<0,true,true>(regb + 12288, vK[0], rK, 16384u);
  mfmab<false>(acc[1][1], Wf[11][1], B[1][1]);
  wait_vm<13>();
  Kf[0][0] = ld128(lds, aK[0] + 16384);
  mfmab<false>(acc[1][2], Wf[11][1], B[1][2]);
  Kf[0][1] = ld128(lds, aK[1] + 16384);
  mfmab<false>(acc[1][3], Wf[11][1], B[1][3]);
  Kf[0][2] = ld128(lds, aK[2] + 16384);
  mfmab<false>(acc[1][4], Wf[11][1], B[1][4]);
  Kf[0][3] = ld128(lds, aK[3] + 16384);
  mfmab<false>(acc[2][0], Wf[11][2], B[1][0]);
  mfmab<false>(acc[2][1], Wf[11][2], B[1][1]);
  dma<1024,true,false>(regb + 12288, vK[1], rK, 16384u);
  mfmab<false>(acc[2][2], Wf[11][2], B[1][2]);
  mfmab<false>(acc[2][3], Wf[11][2], B[1][3]);
  mfmab<false>(acc[2][4], Wf[11][2], B[1][4]);
  mfmab<false>(acc[3][0], Wf[11][3], B[1][0]);
  mfmab<false>(acc[3][1], Wf[11][3], B[1][1]);
  mfmab<false>(acc[3][2], Wf[11][3], B[1][2]);
  dma<2048,true,false>(regb + 12288, vK[2], rK, 16384u);
  mfmab<false>(acc[3][3], Wf[11][3], B[1][3]);
  mfmab<false>(acc[3][4], Wf[11][3], B[1][4]);
  wait_lgkm0();
  mfmab<false>(acc[0][0], Wf[12][0], B[0][0]);
  mfmab<false>(acc[0][1], Wf[12][0], B[0][1]);
  B[1][0] = ld128(lds, aX[1] + 126976);
  mfmab<false>(acc[0][2], Wf[12][0], B[0][2]);
  B[1][1] = ld128(lds, aX[1] + 129024);
  mfmab<false>(acc[0][3], Wf[12][0], B[0][3]);
  B[1][2] = ld128(lds, aX[1] + 153600);
  dma<3072,true,false>(regb + 12288, vK[3], rK, 16384u);
  mfmab<false>(acc[0][4], Wf[12][0], B[0][4]);
  B[1][3] = ld128(lds, aX[1] + 155648);
  mfmab<false>(acc[1][0], Wf[12][1], B[0][0]);
  B[1][4] = ld128(lds, aX[1] + 157696);
  mfmab<false>(acc[1][1], Wf[12][1], B[0][1]);
  wait_vm<12>();
  Kf[1][0] = ld128(lds, aK[0] + 0);
  mfmab<false>(acc[1][2], Wf[12][1], B[0][2]);
  Kf[1][1] = ld128(lds, aK[1] + 0);
  mfmab<false>(acc[1][3], Wf[12][1], B[0][3]);
  Kf[1][2] = ld128(lds, aK[2] + 0);
  mfmab<false>(acc[1][4], Wf[12][1], B[0][4]);
  Kf[1][3] = ld128(lds, aK[3] + 0);
  dma<0,true,true>(regb + 16384, vK[0], rK, 16640u);
  mfmab<false>(acc[2][0], Wf[12][2], B[0][0]);
  mfmab<false>(acc[2][1], Wf[12][2], B[0][1]);
  mfmab<false>(acc[2][2], Wf[12][2], B[0][2]);
  mfmab<false>(acc[2][3], Wf[12][2], B[0][3]);
  mfmab<false>(acc[2][4], Wf[12][2], B[0][4]);
  mfmab<false>(acc[3][0], Wf[12][3], B[0][0]);
  dma<1024,true,false>(regb + 16384, vK[1], rK, 16640u);
  mfmab<false>(acc[3][1], Wf[12][3], B[0][1]);
  mfmab<false>(acc[3][2], Wf[12][3], B[0][2]);
  mfmab<false>(acc[3][3], Wf[12][3], B[0][3]);
  mfmab<false>(acc[3][4], Wf[12][3], B[0][4]);
  wait_lgkm0();
  mfmab<false>(acc[0][0], Wf[13][0], B[1][0]);
  mfmab<false>(acc[1][0], Wf[13][1], B[1][0]);
  dma<2048,true,false>(regb + 16384, vK[2], rK, 16640u);
  mfmab<false>(acc[2][0], Wf[13][2], B[1][0]);
  mfmab<false>(acc[3][0], Wf[13][3], B[1][0]);
  mfmab<false>(acc[0][1], Wf[13][0], B[1][1]);
  mfmab<false>(acc[1][1], Wf[13][1], B[1][1]);
  mfmab<false>(acc[2][1], Wf[13][2], B[1][1]);
  mfmab<false>(acc[3][1], Wf[13][3], B[1][1]);
  pk[0] = vcvt(acc[0][0][0], acc[0][0][1]);
  pk[1] = vcvt(acc[0][0][2], acc[0][0][3]);
  st64(lds, aQ + 0, pk + 0);
  dma<3072,true,false>(regb + 16384, vK[3], rK, 16640u);
  mfmab<false>(acc[0][2], Wf[13][0], B[1][2]);
  pk[2] = vcvt(acc[1][0][0], acc[1][0][1]);
  pk[3] = vcvt(acc[1][0][2], acc[1][0][3]);
  st64(lds, aQ + 512, pk + 2);
  mfmab<false>(acc[1][2], Wf[13][1], B[1][2]);
  pk[0] = vcvt(acc[2][0][0], acc[2][0][1]);
  pk[1] = vcvt(acc[2][0][2], acc[2][0][3]);
  st64(lds, aQ + 5120, pk + 0);
  mfmab<false>(acc[2][2], Wf[13][2], B[1][2]);
  pk[2] = vcvt(acc[3][0][0], acc[3][0][1]);
  pk[3] = vcvt(acc[3][0][2], acc[3][0][3]);
  st64(lds, aQ + 5632, pk + 2);
  mfmab<false>(acc[3][2], Wf[13][3], B[1][2]);
  pk[0] = vcvt(acc[0][1][0], acc[0][1][1]);
  pk[1] = vcvt(acc[0][1][2], acc[0][1][3]);
  st64(lds, aQ + 1024, pk + 0);
  mfmab<false>(acc[0][3], Wf[13][0], B[1][3]);
  pk[2] = vcvt(acc[1][1][0], acc[1][1][1]);
  pk[3] = vcvt(acc[1][1][2], acc[1][1][3]);
  st64(lds, aQ + 1536, pk + 2);
  dma<0,true,true>(regb + 0, vK[0], rK, 24576u);
  mfmab<false>(acc[1][3], Wf[13][1], B[1][3]);
  pk[0] = vcvt(acc[2][1][0], acc[2][1][1]);
  pk[1] = vcvt(acc[2][1][2], acc[2][1][3]);
  st64(lds, aQ + 6144, pk + 0);
  mfmab<false>(acc[2][3], Wf[13][2], B[1][3]);
  pk[2] = vcvt(acc[3][1][0], acc[3][1][1]);
  pk[3] = vcvt(acc[3][1][2], acc[3][1][3]);
  st64(lds, aQ + 6656, pk + 2);
  mfmab<false>(acc[3][3], Wf[13][3], B[1][3]);
  pk[0] = vcvt(acc[0][2][0], acc[0][2][1]);
  pk[1] = vcvt(acc[0][2][2], acc[0][2][3]);
  st64(lds, aQ + 2048, pk + 0);
  mfmab<false>(acc[0][4], Wf[13][0], B[1][4]);
  pk[2] = vcvt(acc[1][2][0], acc[1][2][1]);
  pk[3] = vcvt(acc[1][2][2], acc[1][2][3]);
  st64(lds, aQ + 2560, pk + 2);
  mfmab<false>(acc[1][4], Wf[13][1], B[1][4]);
  pk[0] = vcvt(acc[2][2][0], acc[2][2][1]);
  pk[1] = vcvt(acc[2][2][2], acc[2][2][3]);
  st64(lds, aQ + 7168, pk + 0);
  dma<1024,true,false>(regb + 0, vK[1], rK, 24576u);
  mfmab<false>(acc[2][4], Wf[13][2], B[1][4]);
  pk[2] = vcvt(acc[3][2][0], acc[3][2][1]);
  pk[3] = vcvt(acc[3][2][2], acc[3][2][3]);
  st64(lds, aQ + 7680, pk + 2);
  mfmab<false>(acc[3][4], Wf[13][3], B[1][4]);
  pk[0] = vcvt(acc[0][3][0], acc[0][3][1]);
  pk[1] = vcvt(acc[0][3][2], acc[0][3][3]);
  st64(lds, aQ + 3072, pk + 0);
  mfma_pad();
  pk[2] = vcvt(acc[1][3][0], acc[1][3][1]);
  pk[3] = vcvt(acc[1][3][2], acc[1][3][3]);
  st64(lds, aQ + 3584, pk + 2);
  pk[0] = vcvt(acc[2][3][0], acc[2][3][1]);
  pk[1] = vcvt(acc[2][3][2], acc[2][3][3]);
  st64(lds, aQ + 8192, pk + 0);
  pk[2] = vcvt(acc[3][3][0], acc[3][3][1]);
  pk[3] = vcvt(acc[3][3][2], acc[3][3][3]);
  st64(lds, aQ + 8704, pk + 2);
  pk[0] = vcvt(acc[0][4][0], acc[0][4][1]);
  pk[1] = vcvt(acc[0][4][2], acc[0][4][3]);
  st64(lds, aQ + 4096, pk + 0);
  pk[2] = vcvt(acc[1][4][0], acc[1][4][1]);
  pk[3] = vcvt(acc[1][4][2], acc[1][4][3]);
  st64(lds, aQ + 4608, pk + 2);
  pk[0] = vcvt(acc[2][4][0], acc[2][4][1]);
  dma<2048,true,false>(regb + 0, vK[2], rK, 24576u);
  pk[1] = vcvt(acc[2][4][2], acc[2][4][3]);
  st64(lds, aQ + 9216, pk + 0);
  pk[2] = vcvt(acc[3][4][0], acc[3][4][1]);
  pk[3] = vcvt(acc[3][4][2], acc[3][4][3]);
  st64(lds, aQ + 9728, pk + 2);
  barrier();
  F[0][0] = ld128(lds, aB + 0);
  F[0][1] = ld128(lds, aB + 1024);
  F[0][2] = ld128(lds, aB + 2048);
  F[0][3] = ld128(lds, aB + 3072);
  F[0][4] = ld128(lds, aB + 4096);
  dma<3072,true,false>(regb + 0, vK[3], rK, 24576u);
  mfmav<true>(Sc[0][0], Kf[0][0], F[0][0]);
  F[1][0] = ld128(lds, aB + 5120);
  F[1][1] = ld128(lds, aB + 6144);
  dma<0,true,true>(regb + 20480, vK[0], rK, 24832u);
  mfmav<true>(Sc[0][1], Kf[0][0], F[0][1]);
  F[1][2] = ld128(lds, aB + 7168);
  F[1][3] = ld128(lds, aB + 8192);
  mfmav<true>(Sc[0][2], Kf[0][0], F[0][2]);
  F[1][4] = ld128(lds, aB + 9216);
  F[2][0] = ld128(lds, aB + 10240);
  mfmav<true>(Sc[0][3], Kf[0][0], F[0][3]);
  F[2][1] = ld128(lds, aB + 11264);
  F[2][2] = ld128(lds, aB + 12288);
  mfmav<true>(Sc[0][4], Kf[0][0], F[0][4]);
  F[2][3] = ld128(lds, aB + 13312);
  F[2][4] = ld128(lds, aB + 14336);
  dma<1024,true,false>(regb + 20480, vK[1], rK, 24832u);
  mfmav<false>(Sc[0][0], Kf[0][1], F[1][0]);
  F[3][0] = ld128(lds, aB + 15360);
  F[3][1] = ld128(lds, aB + 16384);
  mfmav<false>(Sc[0][1], Kf[0][1], F[1][1]);
  F[3][2] = ld128(lds, aB + 17408);
  F[3][3] = ld128(lds, aB + 18432);
  mfmav<false>(Sc[0][2], Kf[0][1], F[1][2]);
  F[3][4] = ld128(lds, aB + 19456);
  F[4][0] = ld128(lds, aB + 20480);
  mfmav<false>(Sc[0][3], Kf[0][1], F[1][3]);
  F[4][1] = ld128(lds, aB + 21504);
  F[4][2] = ld128(lds, aB + 22528);
  mfmav<false>(Sc[0][4], Kf[0][1], F[1][4]);
  F[4][3] = ld128(lds, aB + 23552);
  F[4][4] = ld128(lds, aB + 24576);
  mfmav<false>(Sc[0][0], Kf[0][2], F[2][0]);
  F[5][0] = ld128(lds, aB + 25600);
  F[5][1] = ld128(lds, aB + 26624);
  dma<2048,true,false>(regb + 20480, vK[2], rK, 24832u);
  mfmav<false>(Sc[0][1], Kf[0][2], F[2][1]);
  F[5][2] = ld128(lds, aB + 27648);
  F[5][3] = ld128(lds, aB + 28672);
  mfmav<false>(Sc[0][2], Kf[0][2], F[2][2]);
  F[5][4] = ld128(lds, aB + 29696);
  mfmav<false>(Sc[0][3], Kf[0][2], F[2][3]);
  wait_vm<19>();
  Kf[2][0] = ld128(lds, aK[0] + 8192);
  Kf[2][1] = ld128(lds, aK[1] + 8192);
  mfmav<false>(Sc[0][4], Kf[0][2], F[2][4]);
  Kf[2][2] = ld128(lds, aK[2] + 8192);
  mfmav<false>(Sc[0][0], Kf[0][3], F[3][0]);
  Kf[2][3] = ld128(lds, aK[3] + 8192);
  mfmav<false>(Sc[0][1], Kf[0][3], F[3][1]);
  dma<3072,true,false>(regb + 20480, vK[3], rK, 24832u);
  mfmav<false>(Sc[0][2], Kf[0][3], F[3][2]);
  mfmav<false>(Sc[0][3], Kf[0][3], F[3][3]);
  mfmav<false>(Sc[0][4], Kf[0][3], F[3][4]);
  wait_lgkm0();
  mfmav<false>(Sc[0][0], Kf[1][0], F[4][0]);
  F[6][0] = ld128(lds, aB + 30720);
  F[6][1] = ld128(lds, aB + 31744);
  mfmav<false>(Sc[0][1], Kf[1][0], F[4][1]);
  F[6][2] = ld128(lds, aB + 32768);
  F[6][3] = ld128(lds, aB + 33792);
  mfmav<false>(Sc[0][2], Kf[1][0], F[4][2]);
  F[6][4] = ld128(lds, aB + 34816);
  F[7][0] = ld128(lds, aB + 35840);
  dma<0,true,true>(regb + 8192, vK[0], rK, 32768u);
  mfmav<false>(Sc[0][3], Kf[1][0], F[4][3]);
  F[7][1] = ld128(lds, aB + 36864);
  F[7][2] = ld128(lds, aB + 37888);
  mfmav<false>(Sc[0][4], Kf[1][0], F[4][4]);
  F[7][3] = ld128(lds, aB + 38912);
  F[7][4] = ld128(lds, aB + 39936);
  mfmav<false>(Sc[0][0], Kf[1][1], F[5][0]);
  wait_vm<17>();
  Kf[3][0] = ld128(lds, aK[0] + 4096);
  mfmav<false>(Sc[0][1], Kf[1][1], F[5][1]);
  Kf[3][1] = ld128(lds, aK[1] + 4096);
  mfmav<false>(Sc[0][2], Kf[1][1], F[5][2]);
  Kf[3][2] = ld128(lds, aK[2] + 4096);
  mfmav<false>(Sc[0][3], Kf[1][1], F[5][3]);
  Kf[3][3] = ld128(lds, aK[3] + 4096);
  dma<1024,true,false>(regb + 8192, vK[1], rK, 32768u);
  mfmav<false>(Sc[0][4], Kf[1][1], F[5][4]);
  wait_vm<14>();
  Kf[4][0] = ld128(lds, aK[0] + 12288);
  mfmav<false>(Sc[0][0], Kf[1][2], F[6][0]);
  Kf[4][1] = ld128(lds, aK[1] + 12288);
  mfmav<false>(Sc[0][1], Kf[1][2], F[6][1]);
  Kf[4][2] = ld128(lds, aK[2] + 12288);
  mfmav<false>(Sc[0][2], Kf[1][2], F[6][2]);
  Kf[4][3] = ld128(lds, aK[3] + 12288);
  mfmav<false>(Sc[0][3], Kf[1][2], F[6][3]);
  mfmav<false>(Sc[0][4], Kf[1][2], F[6][4]);
  dma<2048,true,false>(regb + 8192, vK[2], rK, 32768u);
  mfmav<false>(Sc[0][0], Kf[1][3], F[7][0]);
  mfmav<false>(Sc[0][1], Kf[1][3], F[7][1]);
  mfmav<false>(Sc[0][2], Kf[1][3], F[7][2]);
  mfmav<false>(Sc[0][3], Kf[1][3], F[7][3]);
  mfmav<false>(Sc[0][4], Kf[1][3], F[7][4]);
  wait_lgkm0();
  mfmav<true>(Sc[1][0], Kf[2][0], F[0][0]);
  dma<3072,true,false>(regb + 8192, vK[3], rK, 32768u);
  mfmav<true>(Sc[1][1], Kf[2][0], F[0][1]);
  wait_vm<12>();
  Kf[5][0] = ld128(lds, aK[0] + 16384);
  mfmav<true>(Sc[1][2], Kf[2][0], F[0][2]);
  Kf[5][1] = ld128(lds, aK[1] + 16384);
  mfmav<true>(Sc[1][3], Kf[2][0], F[0][3]);
  Kf[5][2] = ld128(lds, aK[2] + 16384);
  mfmav<true>(Sc[1][4], Kf[2][0], F[0][4]);
  Kf[5][3] = ld128(lds, aK[3] + 16384);
  mfmav<false>(Sc[1][0], Kf[2][1], F[1][0]);
  mfmav<false>(Sc[1][1], Kf[2][1], F[1][1]);
  mx[0] = vmax3(Sc[0][0][0], Sc[0][0][1], Sc[0][0][1]);
  dma<0,true,true>(regb + 4096, vK[0], rK, 33024u);
  mfmav<false>(Sc[1][2], Kf[2][1], F[1][2]);
  mfmav<false>(Sc[1][3], Kf[2][1], F[1][3]);
  mx[1] = vmax3(Sc[0][1][0], Sc[0][1][1], Sc[0][1][1]);
  mfmav<false>(Sc[1][4], Kf[2][1], F[1][4]);
  mx[2] = vmax3(Sc[0][2][0], Sc[0][2][1], Sc[0][2][1]);
  mfmav<false>(Sc[1][0], Kf[2][2], F[2][0]);
  mfmav<false>(Sc[1][1], Kf[2][2], F[2][1]);
  mx[3] = vmax3(Sc[0][3][0], Sc[0][3][1], Sc[0][3][1]);
  mfmav<false>(Sc[1][2], Kf[2][2], F[2][2]);
  mx[4] = vmax3(Sc[0][4][0], Sc[0][4][1], Sc[0][4][1]);
  dma<1024,true,false>(regb + 4096, vK[1], rK, 33024u);
  mfmav<false>(Sc[1][3], Kf[2][2], F[2][3]);
  mx[0] = vmax3(mx[0], Sc[0][0][2], Sc[0][0][3]);
  mfmav<false>(Sc[1][4], Kf[2][2], F[2][4]);
  mfmav<false>(Sc[1][0], Kf[2][3], F[3][0]);
  mx[1] = vmax3(mx[1], Sc[0][1][2], Sc[0][1][3]);
  mfmav<false>(Sc[1][1], Kf[2][3], F[3][1]);
  mx[2] = vmax3(mx[2], Sc[0][2][2], Sc[0][2][3]);
  mfmav<false>(Sc[1][2], Kf[2][3], F[3][2]);
  mx[3] = vmax3(mx[3], Sc[0][3][2], Sc[0][3][3]);
  mfmav<false>(Sc[1][3], Kf[2][3], F[3][3]);
  dma<2048,true,false>(regb + 4096, vK[2], rK, 33024u);
  mfmav<false>(Sc[1][4], Kf[2][3], F[3][4]);
  mx[4] = vmax3(mx[4], Sc[0][4][2], Sc[0][4][3]);
  wait_lgkm0();
  mfmav<false>(Sc[1][0], Kf[3][0], F[4][0]);
  mfmav<false>(Sc[1][1], Kf[3][0], F[4][1]);
  wait_vm<11>();
  Kf[6][0] = ld128(lds, aK[0] + 0);
  mfmav<false>(Sc[1][2], Kf[3][0], F[4][2]);
  Kf[6][1] = ld128(lds, aK[1] + 0);
  mfmav<false>(Sc[1][3], Kf[3][0], F[4][3]);
  Kf[6][2] = ld128(lds, aK[2] + 0);
  mfmav<false>(Sc[1][4], Kf[3][0], F[4][4]);
  Kf[6][3] = ld128(lds, aK[3] + 0);
  dma<3072,true,false>(regb + 4096, vK[3], rK, 33024u);
  mfmav<false>(Sc[1][0], Kf[3][1], F[5][0]);
  mfmav<false>(Sc[1][1], Kf[3][1], F[5][1]);
  mfmav<false>(Sc[1][2], Kf[3][1], F[5][2]);
  mfmav<false>(Sc[1][3], Kf[3][1], F[5][3]);
  mfmav<false>(Sc[1][4], Kf[3][1], F[5][4]);
  mfmav<false>(Sc[1][0], Kf[3][2], F[6][0]);
  dma<0,true,true>(regb + 12288, vK[0], rK, 40960u);
  mfmav<false>(Sc[1][1], Kf[3][2], F[6][1]);
  mfmav<false>(Sc[1][2], Kf[3][2], F[6][2]);
  mfmav<false>(Sc[1][3], Kf[3][2], F[6][3]);
  mfmav<false>(Sc[1][4], Kf[3][2], F[6][4]);
  mfmav<false>(Sc[1][0], Kf[3][3], F[7][0]);
  mfmav<false>(Sc[1][1], Kf[3][3], F[7][1]);
  dma<1024,true,false>(regb + 12288, vK[1], rK, 40960u);
  mfmav<false>(Sc[1][2], Kf[3][3], F[7][2]);
  mfmav<false>(Sc[1][3], Kf[3][3], F[7][3]);
  mfmav<false>(Sc[1][4], Kf[3][3], F[7][4]);
  wait_lgkm0();
  mfmav<true>(Sc[2][0], Kf[4][0], F[0][0]);
  mfmav<true>(Sc[2][1], Kf[4][0], F[0][1]);
  mfmav<true>(Sc[2][2], Kf[4][0], F[0][2]);
  mx[0] = vmax3(mx[0], Sc[1][0][0], Sc[1][0][1]);
  dma<2048,true,false>(regb + 12288, vK[2], rK, 40960u);
  mfmav<true>(Sc[2][3], Kf[4][0], F[0][3]);
  mx[1] = vmax3(mx[1], Sc[1][1][0], Sc[1][1][1]);
  mx[2] = vmax3(mx[2], Sc[1][2][0], Sc[1][2][1]);
  mfmav<true>(Sc[2][4], Kf[4][0], F[0][4]);
  mx[3] = vmax3(mx[3], Sc[1][3][0], Sc[1][3][1]);
  mfmav<false>(Sc[2][0], Kf[4][1], F[1][0]);
  mx[4] = vmax3(mx[4], Sc[1][4][0], Sc[1][4][1]);
  mfmav<false>(Sc[2][1], Kf[4][1], F[1][1]);
  mx[0] = vmax3(mx[0], Sc[1][0][2], Sc[1][0][3]);
  mfmav<false>(Sc[2][2], Kf[4][1], F[1][2]);
  mx[1] = vmax3(mx[1], Sc[1][1][2], Sc[1][1][3]);
  mx[2] = vmax3(mx[2], Sc[1][2][2], Sc[1][2][3]);
  mfmav<false>(Sc[2][3], Kf[4][1], F[1][3]);
  mx[3] = vmax3(mx[3], Sc[1][3][2], Sc[1][3][3]);
  dma<3072,true,false>(regb + 12288, vK[3], rK, 40960u);
  mfmav<false>(Sc[2][4], Kf[4][1], F[1][4]);
  mx[4] = vmax3(mx[4], Sc[1][4][2], Sc[1][4][3]);
  mfmav<false>(Sc[2][0], Kf[4][2], F[2][0]);
  SWAP16(0);
  mfmav<false>(Sc[2][1], Kf[4][2], F[2][1]);
  mfmav<false>(Sc[2][2], Kf[4][2], F[2][2]);
  SWAP16(1);
  mfmav<false>(Sc[2][3], Kf[4][2], F[2][3]);
  SWAP16(2);
  mfmav<false>(Sc[2][4], Kf[4][2], F[2][4]);
  dma<0,true,true>(regb + 0, vK[0], rK, 41216u);
  mfmav<false>(Sc[2][0], Kf[4][3], F[3][0]);
  SWAP16(3);
  mfmav<false>(Sc[2][1], Kf[4][3], F[3][1]);
  SWAP16(4);
  mfmav<false>(Sc[2][2], Kf[4][3], F[3][2]);
  mfmav<false>(Sc[2][3], Kf[4][3], F[3][3]);
  mx[0] = vmax3(t0_[0], t1_[0], t1_[0]);
  mx[1] = vmax3(t0_[1], t1_[1], t1_[1]);
  mfmav<false>(Sc[2][4], Kf[4][3], F[3][4]);
  mx[2] = vmax3(t0_[2], t1_[2], t1_[2]);
  wait_lgkm0();
  mfmav<false>(Sc[2][0], Kf[5][0], F[4][0]);
  mx[3] = vmax3(t0_[3], t1_[3], t1_[3]);
  dma<1024,true,false>(regb + 0, vK[1], rK, 41216u);
  mfmav<false>(Sc[2][1], Kf[5][0], F[4][1]);
  wait_vm<14>();
  Kf[7][0] = ld128(lds, aK[0] + 20480);
  mx[4] = vmax3(t0_[4], t1_[4], t1_[4]);
  mfmav<false>(Sc[2][2], Kf[5][0], F[4][2]);
  Kf[7][1] = ld128(lds, aK[1] + 20480);
  mfmav<false>(Sc[2][3], Kf[5][0], F[4][3]);
  Kf[7][2] = ld128(lds, aK[2] + 20480);
  mfmav<false>(Sc[2][4], Kf[5][0], F[4][4]);
  Kf[7][3] = ld128(lds, aK[3] + 20480);
  mfmav<false>(Sc[2][0], Kf[5][1], F[5][0]);
  SWAP32(0);
  mfmav<false>(Sc[2][1], Kf[5][1], F[5][1]);
  SWAP32(1);
  dma<2048,true,false>(regb + 0, vK[2], rK, 41216u);
  mfmav<false>(Sc[2][2], Kf[5][1], F[5][2]);
  mfmav<false>(Sc[2][3], Kf[5][1], F[5][3]);
  SWAP32(2);
  mfmav<false>(Sc[2][4], Kf[5][1], F[5][4]);
  SWAP32(3);
  mfmav<false>(Sc[2][0], Kf[5][2], F[6][0]);
  mfmav<false>(Sc[2][1], Kf[5][2], F[6][1]);
  SWAP32(4);
  mfmav<false>(Sc[2][2], Kf[5][2], F[6][2]);
  mx[0] = vmax3(t0_[0], t1_[0], t1_[0]);
  dma<3072,true,false>(regb + 0, vK[3], rK, 41216u);
  mfmav<false>(Sc[2][3], Kf[5][2], F[6][3]);
  mx[1] = vmax3(t0_[1], t1_[1], t1_[1]);
  mfmav<false>(Sc[2][4], Kf[5][2], F[6][4]);
  mx[2] = vmax3(t0_[2], t1_[2], t1_[2]);
  mx[3] = vmax3(t0_[3], t1_[3], t1_[3]);
  mfmav<false>(Sc[2][0], Kf[5][3], F[7][0]);
  mx[4] = vmax3(t0_[4], t1_[4], t1_[4]);
  mfmav<false>(Sc[2][1], Kf[5][3], F[7][1]);
  *(LAS float*)(lds + aS + ST_MAX + 0) = mx[0];
  mfmav<false>(Sc[2][2], Kf[5][3], F[7][2]);
  *(LAS float*)(lds + aS + ST_MAX + 256) = mx[1];
  mfmav<false>(Sc[2][3], Kf[5][3], F[7][3]);
  *(LAS float*)(lds + aS + ST_MAX + 512) = mx[2];
  *(LAS float*)(lds + aS + ST_MAX + 768) = mx[3];
  dma<0,true,true>(regb + 16384, vK[0], rK, 49152u);
  mfmav<false>(Sc[2][4], Kf[5][3], F[7][4]);
  *(LAS float*)(lds + aS + ST_MAX + 1024) = mx[4];
  barrier();
  mfmav<true>(Sc[3][0], Kf[6][0], F[0][0]);
  mm[0] = *(LAS f32x4*)(lds + aSr + ST_MAX + 0);
  mm[1] = *(LAS f32x4*)(lds + aSr + ST_MAX + 256);
  mfmav<true>(Sc[3][1], Kf[6][0], F[0][1]);
  wait_vm<13>();
  Kf[8][0] = ld128(lds, aK[0] + 8192);
  mm[2] = *(LAS f32x4*)(lds + aSr + ST_MAX + 512);
  mm[3] = *(LAS f32x4*)(lds + aSr + ST_MAX + 768);
  mfmav<true>(Sc[3][2], Kf[6][0], F[0][2]);
  Kf[8][1] = ld128(lds, aK[1] + 8192);
  mm[4] = *(LAS f32x4*)(lds + aSr + ST_MAX + 1024);
  dma<1024,true,false>(regb + 16384, vK[1], rK, 49152u);
  mfmav<true>(Sc[3][3], Kf[6][0], F[0][3]);
  Kf[8][2] = ld128(lds, aK[2] + 8192);
  mfmav<true>(Sc[3][4], Kf[6][0], F[0][4]);
  Kf[8][3] = ld128(lds, aK[3] + 8192);
  mfmav<false>(Sc[3][0], Kf[6][1], F[1][0]);
  t0_[0] = vmax3(mm[0][0], mm[0][1], mm[0][2]);
  t0_[1] = vmax3(mm[1][0], mm[1][1], mm[1][2]);
  mfmav<false>(Sc[3][1], Kf[6][1], F[1][1]);
  t0_[2] = vmax3(mm[2][0], mm[2][1], mm[2][2]);
  t0_[3] = vmax3(mm[3][0], mm[3][1], mm[3][2]);
  mfmav<false>(Sc[3][2], Kf[6][1], F[1][2]);
  t0_[4] = vmax3(mm[4][0], mm[4][1], mm[4][2]);
  mA[0] = vmax3(t0_[0], mm[0][3], mm[0][3]);
  dma<2048,true,false>(regb + 16384, vK[2], rK, 49152u);
  mfmav<false>(Sc[3][3], Kf[6][1], F[1][3]);
  mA[1] = vmax3(t0_[1], mm[1][3], mm[1][3]);
  mA[2] = vmax3(t0_[2], mm[2][3], mm[2][3]);
  mfmav<false>(Sc[3][4], Kf[6][1], F[1][4]);
  mA[3] = vmax3(t0_[3], mm[3][3], mm[3][3]);
  mA[4] = vmax3(t0_[4], mm[4][3], mm[4][3]);
  mfmav<false>(Sc[3][0], Kf[6][2], F[2][0]);
  nmA[0] = vmul(mA[0], ncs);
  nmA[1] = vmul(mA[1], ncs);
  mfmav<false>(Sc[3][1], Kf[6][2], F[2][1]);
  nmA[2] = vmul(mA[2], ncs);
  nmA[3] = vmul(mA[3], ncs);
  mfmav<false>(Sc[3][2], Kf[6][2], F[2][2]);
  nmA[4] = vmul(mA[4], ncs);
  y[0][0] = vfma(Sc[0][0][0], nmA[0]);
  y[0][1] = vfma(Sc[0][0][1], nmA[0]);
  mfmav<false>(Sc[3][3], Kf[6][2], F[2][3]);
  y[0][2] = vfma(Sc[0][0][2], nmA[0]);
  y[0][3] = vfma(Sc[0][0][3], nmA[0]);
  dma<3072,true,false>(regb + 16384, vK[3], rK, 49152u);
  mfmav<false>(Sc[3][4], Kf[6][2], F[2][4]);
  y[0][4] = vfma(Sc[1][0][0], nmA[0]);
  y[0][5] = vfma(Sc[1][0][1], nmA[0]);
  mfmav<false>(Sc[3][0], Kf[6][3], F[3][0]);
  mx[0] = vmax3(Sc[2][0][0], Sc[2][0][1], Sc[2][0][1]);
  mx[1] = vmax3(Sc[2][1][0], Sc[2][1][1], Sc[2][1][1]);
  mfmav<false>(Sc[3][1], Kf[6][3], F[3][1]);
  mx[2] = vmax3(Sc[2][2][0], Sc[2][2][1], Sc[2][2][1]);
  mx[3] = vmax3(Sc[2][3][0], Sc[2][3][1], Sc[2][3][1]);
  mfmav<false>(Sc[3][2], Kf[6][3], F[3][2]);
  mx[4] = vmax3(Sc[2][4][0], Sc[2][4][1], Sc[2][4][1]);
  mx[0] = vmax3(mx[0], Sc[2][0][2], Sc[2][0][3]);
  mfmav<false>(Sc[3][3], Kf[6][3], F[3][3]);
  mx[1] = vmax3(mx[1], Sc[2][1][2], Sc[2][1][3]);
  mx[2] = vmax3(mx[2], Sc[2][2][2], Sc[2][2][3]);
  mfmav<false>(Sc[3][4], Kf[6][3], F[3][4]);
  mx[3] = vmax3(mx[3], Sc[2][3][2], Sc[2][3][3]);
  mx[4] = vmax3(mx[4], Sc[2][4][2], Sc[2][4][3]);
  dma<0,true,true>(regb + 20480, vK[0], rK, 49408u);
  wait_lgkm0();
  mfmav<false>(Sc[3][0], Kf[7][0], F[4][0]);
  y[0][6] = vfma(Sc[1][0][2], nmA[0]);
  y[0][7] = vfma(Sc[1][0][3], nmA[0]);
  mfmav<false>(Sc[3][1], Kf[7][0], F[4][1]);
  wait_vm<13>();
  Kf[9][0] = ld128(lds, aK[0] + 4096);
  mfmav<false>(Sc[3][2], Kf[7][0], F[4][2]);
  Kf[9][1] = ld128(lds, aK[1] + 4096);
  mfmav<false>(Sc[3][3], Kf[7][0], F[4][3]);
  Kf[9][2] = ld128(lds, aK[2] + 4096);
  vexp(y[0][0]);
  mfmav<false>(Sc[3][4], Kf[7][0], F[4][4]);
  Kf[9][3] = ld128(lds, aK[3] + 4096);
  dma<1024,true,false>(regb + 20480, vK[1], rK, 49408u);
  mfmav<false>(Sc[3][0], Kf[7][1], F[5][0]);
  y[1][0] = vfma(Sc[0][1][0], nmA[1]);
  mfmav<false>(Sc[3][1], Kf[7][1], F[5][1]);
  vexp(y[0][1]);
  mfmav<false>(Sc[3][2], Kf[7][1], F[5][2]);
  y[1][1] = vfma(Sc[0][1][1], nmA[1]);
  mfmav<false>(Sc[3][3], Kf[7][1], F[5][3]);
  vexp(y[0][2]);
  mfmav<false>(Sc[3][4], Kf[7][1], F[5][4]);
  y[1][2] = vfma(Sc[0][1][2], nmA[1]);
  vexp(y[0][3]);
  mfmav<false>(Sc[3][0], Kf[7][2], F[6][0]);
  y[1][3] = vfma(Sc[0][1][3], nmA[1]);
  dma<2048,true,false>(regb + 20480, vK[2], rK, 49408u);
  mfmav<false>(Sc[3][1], Kf[7][2], F[6][1]);
  vexp(y[0][4]);
  mfmav<false>(Sc[3][2], Kf[7][2], F[6][2]);
  y[1][4] = vfma(Sc[1][1][0], nmA[1]);
  mfmav<false>(Sc[3][3], Kf[7][2], F[6][3]);
  vexp(y[0][5]);
  mfmav<false>(Sc[3][4], Kf[7][2], F[6][4]);
  y[1][5] = vfma(Sc[1][1][1], nmA[1]);
  mfmav<false>(Sc[3][0], Kf[7][3], F[7][0]);
  vexp(y[0][6]);
  mfmav<false>(Sc[3][1], Kf[7][3], F[7][1]);
  y[1][6] = vfma(Sc[1][1][2], nmA[1]);
  dma<3072,true,false>(regb + 20480, vK[3], rK, 49408u);
  mfmav<false>(Sc[3][2], Kf[7][3], F[7][2]);
  vexp(y[0][7]);
  mfmav<false>(Sc[3][3], Kf[7][3], F[7][3]);
  y[1][7] = vfma(Sc[1][1][3], nmA[1]);
  mfmav<false>(Sc[3][4], Kf[7][3], F[7][4]);
  vexp(y[1][0]);
  wait_lgkm0();
  mfmav<true>(Sc[4][0], Kf[8][0], F[0][0]);
  mfmav<true>(Sc[4][1], Kf[8][0], F[0][1]);
  wait_vm<12>();
  Kf[10][0] = ld128(lds, aK[0] + 12288);
  mfmav<true>(Sc[4][2], Kf[8][0], F[0][2]);
  Kf[10][1] = ld128(lds, aK[1] + 12288);
  ls2[0][0] = y[0][0];
  dma<0,true,true>(regb + 4096, vK[0], rK, 57344u);
  mfmav<true>(Sc[4][3], Kf[8][0], F[0][3]);
  Kf[10][2] = ld128(lds, aK[2] + 12288);
  mfmav<true>(Sc[4][4], Kf[8][0], F[0][4]);
  Kf[10][3] = ld128(lds, aK[3] + 12288);
  mx[0] = vmax3(mx[0], Sc[3][0][0], Sc[3][0][1]);
  mfmav<false>(Sc[4][0], Kf[8][1], F[1][0]);
  vexp(y[1][1]);
  mfmav<false>(Sc[4][1], Kf[8][1], F[1][1]);
  mx[1] = vmax3(mx[1], Sc[3][1][0], Sc[3][1][1]);
  ls2[0][1] = y[0][1];
  mfmav<false>(Sc[4][2], Kf[8][1], F[1][2]);
  mx[2] = vmax3(mx[2], Sc[3][2][0], Sc[3][2][1]);
  mfmav<false>(Sc[4][3], Kf[8][1], F[1][3]);
  vexp(y[1][2]);
  dma<1024,true,false>(regb + 4096, vK[1], rK, 57344u);
  mfmav<false>(Sc[4][4], Kf[8][1], F[1][4]);
  mx[3] = vmax3(mx[3], Sc[3][3][0], Sc[3][3][1]);
  pq[0][0] = vcvt(y[0][0], y[0][1]);
  mfmav<false>(Sc[4][0], Kf[8][2], F[2][0]);
  mx[4] = vmax3(mx[4], Sc[3][4][0], Sc[3][4][1]);
  mfmav<false>(Sc[4][1], Kf[8][2], F[2][1]);
  vexp(y[1][3]);
  mfmav<false>(Sc[4][2], Kf[8][2], F[2][2]);
  mx[0] = vmax3(mx[0], Sc[3][0][2], Sc[3][0][3]);
  vadd(ls2[0][0], y[0][2]);
  mfmav<false>(Sc[4][3], Kf[8][2], F[2][3]);
  mx[1] = vmax3(mx[1], Sc[3][1][2], Sc[3][1][3]);
  mfmav<false>(Sc[4][4], Kf[8][2], F[2][4]);
  vexp(y[1][4]);
  dma<2048,true,false>(regb + 4096, vK[2], rK, 57344u);
  mfmav<false>(Sc[4][0], Kf[8][3], F[3][0]);
  mx[2] = vmax3(mx[2], Sc[3][2][2], Sc[3][2][3]);
  vadd(ls2[0][1], y[0][3]);
  mfmav<false>(Sc[4][1], Kf[8][3], F[3][1]);
  mx[3] = vmax3(mx[3], Sc[3][3][2], Sc[3][3][3]);
  mfmav<false>(Sc[4][2], Kf[8][3], F[3][2]);
  vexp(y[1][5]);
  mx[4] = vmax3(mx[4], Sc[3][4][2], Sc[3][4][3]);
  mfmav<false>(Sc[4][3], Kf[8][3], F[3][3]);
  pq[0][1] = vcvt(y[0][2], y[0][3]);
  mfmav<false>(Sc[4][4], Kf[8][3], F[3][4]);
  vexp(y[1][6]);
  dma<3072,true,false>(regb + 4096, vK[3], rK, 57344u);
  wait_lgkm0();
  mfmav<false>(Sc[4][0], Kf[9][0], F[4][0]);
  vadd(ls2[0][0], y[0][4]);
  mfmav<false>(Sc[4][1], Kf[9][0], F[4][1]);
  wait_vm<12>();
  Kf[11][0] = ld128(lds, aK[0] + 0);
  vexp(y[1][7]);
  mfmav<false>(Sc[4][2], Kf[9][0], F[4][2]);
  Kf[11][1] = ld128(lds, aK[1] + 0);
  mfmav<false>(Sc[4][3], Kf[9][0], F[4][3]);
  Kf[11][2] = ld128(lds, aK[2] + 0);
  vadd(ls2[0][1], y[0][5]);
  mfmav<false>(Sc[4][4], Kf[9][0], F[4][4]);
  Kf[11][3] = ld128(lds, aK[3] + 0);
  mfmav<false>(Sc[4][0], Kf[9][1], F[5][0]);
  pq[0][2] = vcvt(y[0][4], y[0][5]);
  vadd(ls2[0][0], y[0][6]);
  dma<0,true,true>(regb + 8192, vK[0], rK, 57600u);
  mfmav<false>(Sc[4][1], Kf[9][1], F[5][1]);
  vadd(ls2[0][1], y[0][7]);
  pq[0][3] = vcvt(y[0][6], y[0][7]);
  mfmav<false>(Sc[4][2], Kf[9][1], F[5][2]);
  st128(lds, aP + 0, pq[0]);
  mfmav<false>(Sc[4][3], Kf[9][1], F[5][3]);
  y[2][0] = vfma(Sc[0][2][0], nmA[2]);
  mfmav<false>(Sc[4][4], Kf[9][1], F[5][4]);
  y[2][1] = vfma(Sc[0][2][1], nmA[2]);
  mfmav<false>(Sc[4][0], Kf[9][2], F[6][0]);
  y[2][2] = vfma(Sc[0][2][2], nmA[2]);
  y[2][3] = vfma(Sc[0][2][3], nmA[2]);
  mfmav<false>(Sc[4][1], Kf[9][2], F[6][1]);
  y[2][4] = vfma(Sc[1][2][0], nmA[2]);
  y[2][5] = vfma(Sc[1][2][1], nmA[2]);
  dma<1024,true,false>(regb + 8192, vK[1], rK, 57600u);
  mfmav<false>(Sc[4][2], Kf[9][2], F[6][2]);
  y[2][6] = vfma(Sc[1][2][2], nmA[2]);
  mfmav<false>(Sc[4][3], Kf[9][2], F[6][3]);
  y[2][7] = vfma(Sc[1][2][3], nmA[2]);
  vexp(y[2][0]);
  mfmav<false>(Sc[4][4], Kf[9][2], F[6][4]);
  ls2[1][0] = y[1][0];
  mfmav<false>(Sc[4][0], Kf[9][3], F[7][0]);
  vexp(y[2][1]);
  mfmav<false>(Sc[4][1], Kf[9][3], F[7][1]);
  ls2[1][1] = y[1][1];
  dma<2048,true,false>(regb + 8192, vK[2], rK, 57600u);
  mfmav<false>(Sc[4][2], Kf[9][3], F[7][2]);
  vexp(y[2][2]);
  mfmav<false>(Sc[4][3], Kf[9][3], F[7][3]);
  pq[1][0] = vcvt(y[1][0], y[1][1]);
  mfmav<false>(Sc[4][4], Kf[9][3], F[7][4]);
  vexp(y[2][3]);
  wait_lgkm0();
  mfmav<true>(Sc[5][0], Kf[10][0], F[0][0]);
  mfmav<true>(Sc[5][1], Kf[10][0], F[0][1]);
  wait_vm<11>();
  Kf[12][0] = ld128(lds, aK[0] + 16384);
  mfmav<true>(Sc[5][2], Kf[10][0], F[0][2]);
  Kf[12][1] = ld128(lds, aK[1] + 16384);
  vadd(ls2[1][0], y[1][2]);
  dma<3072,true,false>(regb + 8192, vK[3], rK, 57600u);
  mfmav<true>(Sc[5][3], Kf[10][0], F[0][3]);
  Kf[12][2] = ld128(lds, aK[2] + 16384);
  mfmav<true>(Sc[5][4], Kf[10][0], F[0][4]);
  Kf[12][3] = ld128(lds, aK[3] + 16384);
  mx[0] = vmax3(mx[0], Sc[4][0][0], Sc[4][0][1]);
  mfmav<false>(Sc[5][0], Kf[10][1], F[1][0]);
  vexp(y[2][4]);
  mfmav<false>(Sc[5][1], Kf[10][1], F[1][1]);
  mx[1] = vmax3(mx[1], Sc[4][1][0], Sc[4][1][1]);
  mfmav<false>(Sc[5][2], Kf[10][1], F[1][2]);
  vadd(ls2[1][1], y[1][3]);
  mx[2] = vmax3(mx[2], Sc[4][2][0], Sc[4][2][1]);
  mfmav<false>(Sc[5][3], Kf[10][1], F[1][3]);
  vexp(y[2][5]);
  dma<0,true,true>(regb + 0, vV[0], rV, 0u);
  mfmav<false>(Sc[5][4], Kf[10][1], F[1][4]);
  mx[3] = vmax3(mx[3], Sc[4][3][0], Sc[4][3][1]);
  mfmav<false>(Sc[5][0], Kf[10][2], F[2][0]);
  pq[1][1] = vcvt(y[1][2], y[1][3]);
  mx[4] = vmax3(mx[4], Sc[4][4][0], Sc[4][4][1]);
  mfmav<false>(Sc[5][1], Kf[10][2], F[2][1]);
  vexp(y[2][6]);
  mfmav<false>(Sc[5][2], Kf[10][2], F[2][2]);
  mx[0] = vmax3(mx[0], Sc[4][0][2], Sc[4][0][3]);
  mfmav<false>(Sc[5][3], Kf[10][2], F[2][3]);
  vadd(ls2[1][0], y[1][4]);
  mx[1] = vmax3(mx[1], Sc[4][1][2], Sc[4][1][3]);
  mfmav<false>(Sc[5][4], Kf[10][2], F[2][4]);
  vexp(y[2][7]);
  dma<1024,true,false>(regb + 0, vV[1], rV, 0u);
  mfmav<false>(Sc[5][0], Kf[10][3], F[3][0]);
  mx[2] = vmax3(mx[2], Sc[4][2][2], Sc[4][2][3]);
  mfmav<false>(Sc[5][1], Kf[10][3], F[3][1]);
  vadd(ls2[1][1], y[1][5]);
  mx[3] = vmax3(mx[3], Sc[4][3][2], Sc[4][3][3]);
  mfmav<false>(Sc[5][2], Kf[10][3], F[3][2]);
  pq[1][2] = vcvt(y[1][4], y[1][5]);
  mx[4] = vmax3(mx[4], Sc[4][4][2], Sc[4][4][3]);
  mfmav<false>(Sc[5][3], Kf[10][3], F[3][3]);
  vadd(ls2[1][0], y[1][6]);
  mfmav<false>(Sc[5][4], Kf[10][3], F[3][4]);
  vadd(ls2[1][1], y[1][7]);
  pq[1][3] = vcvt(y[1][6], y[1][7]);
  wait_lgkm0();
  mfmav<false>(Sc[5][0], Kf[11][0], F[4][0]);
  st128(lds, aP + 1024, pq[1]);
  dma<2048,true,false>(regb + 0, vV[2], rV, 0u);
  mfmav<false>(Sc[5][1], Kf[11][0], F[4][1]);
  y[0][0] = vfma(Sc[0][3][0], nmA[3]);
  mfmav<false>(Sc[5][2], Kf[11][0], F[4][2]);
  y[0][1] = vfma(Sc[0][3][1], nmA[3]);
  mfmav<false>(Sc[5][3], Kf[11][0], F[4][3]);
  y[0][2] = vfma(Sc[0][3][2], nmA[3]);
  y[0][3] = vfma(Sc[0][3][3], nmA[3]);
  mfmav<false>(Sc[5][4], Kf[11][0], F[4][4]);
  y[0][4] = vfma(Sc[1][3][0], nmA[3]);
  mfmav<false>(Sc[5][0], Kf[11][1], F[5][0]);
  y[0][5] = vfma(Sc[1][3][1], nmA[3]);
  dma<3072,true,false>(regb + 0, vV[3], rV, 0u);
  mfmav<false>(Sc[5][1], Kf[11][1], F[5][1]);
  y[0][6] = vfma(Sc[1][3][2], nmA[3]);
  y[0][7] = vfma(Sc[1][3][3], nmA[3]);
  mfmav<false>(Sc[5][2], Kf[11][1], F[5][2]);
  vexp(y[0][0]);
  mfmav<false>(Sc[5][3], Kf[11][1], F[5][3]);
  ls2[2][0] = y[2][0];
  mfmav<false>(Sc[5][4], Kf[11][1], F[5][4]);
  mfmav<false>(Sc[5][0], Kf[11][2], F[6][0]);
  vexp(y[0][1]);
  mfmav<false>(Sc[5][1], Kf[11][2], F[6][1]);
  ls2[2][1] = y[2][1];
  dma<0,true,true>(regb + 12288, vV[0], rV, 65536u);
  mfmav<false>(Sc[5][2], Kf[11][2], F[6][2]);
  vexp(y[0][2]);
  mfmav<false>(Sc[5][3], Kf[11][2], F[6][3]);
  pq[2][0] = vcvt(y[2][0], y[2][1]);
  mfmav<false>(Sc[5][4], Kf[11][2], F[6][4]);
  vexp(y[0][3]);
  mfmav<false>(Sc[5][0], Kf[11][3], F[7][0]);
  vadd(ls2[2][0], y[2][2]);
  mfmav<false>(Sc[5][1], Kf[11][3], F[7][1]);
  vexp(y[0][4]);
  mfmav<false>(Sc[5][2], Kf[11][3], F[7][2]);
  vadd(ls2[2][1], y[2][3]);
  dma<1024,true,false>(regb + 12288, vV[1], rV, 65536u);
  mfmav<false>(Sc[5][3], Kf[11][3], F[7][3]);
  vexp(y[0][5]);
  mfmav<false>(Sc[5][4], Kf[11][3], F[7][4]);
  pq[2][1] = vcvt(y[2][2], y[2][3]);
  wait_lgkm0();
  mfmav<true>(Sc[6][0], Kf[12][0], F[0][0]);
  mfmav<true>(Sc[6][1], Kf[12][0], F[0][1]);
  wait_vm<14>();
  Kf[13][0] = ld128(lds, aK[0] + 20480);
  mfmav<true>(Sc[6][2], Kf[12][0], F[0][2]);
  Kf[13][1] = ld128(lds, aK[1] + 20480);
  mfmav<true>(Sc[6][3], Kf[12][0], F[0][3]);
  Kf[13][2] = ld128(lds, aK[2] + 20480);
  vexp(y[0][6]);
  dma<2048,true,false>(regb + 12288, vV[2], rV, 65536u);
  mfmav<true>(Sc[6][4], Kf[12][0], F[0][4]);
  Kf[13][3] = ld128(lds, aK[3] + 20480);
  mfmav<false>(Sc[6][0], Kf[12][1], F[1][0]);
  mx[0] = vmax3(mx[0], Sc[5][0][0], Sc[5][0][1]);
  vadd(ls2[2][0], y[2][4]);
  mfmav<false>(Sc[6][1], Kf[12][1], F[1][1]);
  mx[1] = vmax3(mx[1], Sc[5][1][0], Sc[5][1][1]);
  mfmav<false>(Sc[6][2], Kf[12][1], F[1][2]);
  vexp(y[0][7]);
  mfmav<false>(Sc[6][3], Kf[12][1], F[1][3]);
  mx[2] = vmax3(mx[2], Sc[5][2][0], Sc[5][2][1]);
  vadd(ls2[2][1], y[2][5]);
  mfmav<false>(Sc[6][4], Kf[12][1], F[1][4]);
  mx[3] = vmax3(mx[3], Sc[5][3][0], Sc[5][3][1]);
  pq[2][2] = vcvt(y[2][4], y[2][5]);
  dma<3072,true,false>(regb + 12288, vV[3], rV, 65536u);
  mfmav<false>(Sc[6][0], Kf[12][2], F[2][0]);
  mx[4] = vmax3(mx[4], Sc[5][4][0], Sc[5][4][1]);
  vadd(ls2[2][0], y[2][6]);
  mfmav<false>(Sc[6][1], Kf[12][2], F[2][1]);
  mx[0] = vmax3(mx[0], Sc[5][0][2], Sc[5][0][3]);
  mfmav<false>(Sc[6][2], Kf[12][2], F[2][2]);
  vadd(ls2[2][1], y[2][7]);
  mx[1] = vmax3(mx[1], Sc[5][1][2], Sc[5][1][3]);
  mfmav<false>(Sc[6][3], Kf[12][2], F[2][3]);
  pq[2][3] = vcvt(y[2][6], y[2][7]);
  mx[2] = vmax3(mx[2], Sc[5][2][2], Sc[5][2][3]);
  mfmav<false>(Sc[6][4], Kf[12][2], F[2][4]);
  st128(lds, aP + 2048, pq[2]);
  mfmav<false>(Sc[6][0], Kf[12][3], F[3][0]);
  mx[3] = vmax3(mx[3], Sc[5][3][2], Sc[5][3][3]);
  dma<0,true,true>(regb + 16384, vV[0], rV, 131072u);
  mfmav<false>(Sc[6][1], Kf[12][3], F[3][1]);
  y[1][0] = vfma(Sc[0][4][0], nmA[4]);
  mx[4] = vmax3(mx[4], Sc[5][4][2], Sc[5][4][3]);
  mfmav<false>(Sc[6][2], Kf[12][3], F[3][2]);
  y[1][1] = vfma(Sc[0][4][1], nmA[4]);
  y[1][2] = vfma(Sc[0][4][2], nmA[4]);
  mfmav<false>(Sc[6][3], Kf[12][3], F[3][3]);
  y[1][3] = vfma(Sc[0][4][3], nmA[4]);
  mfmav<false>(Sc[6][4], Kf[12][3], F[3][4]);
  y[1][4] = vfma(Sc[1][4][0], nmA[4]);
  y[1][5] = vfma(Sc[1][4][1], nmA[4]);
  wait_lgkm0();
  mfmav<false>(Sc[6][0], Kf[13][0], F[4][0]);
  y[1][6] = vfma(Sc[1][4][2], nmA[4]);
  y[1][7] = vfma(Sc[1][4][3], nmA[4]);
  mfmav<false>(Sc[6][1], Kf[13][0], F[4][1]);
  wait_vm<13>();
  Kf[14][0] = ld128(lds, aK[0] + 4096);
  dma<1024,true,false>(regb + 16384, vV[1], rV, 131072u);
  mfmav<false>(Sc[6][2], Kf[13][0], F[4][2]);
  Kf[14][1] = ld128(lds, aK[1] + 4096);
  mfmav<false>(Sc[6][3], Kf[13][0], F[4][3]);
  Kf[14][2] = ld128(lds, aK[2] + 4096);
  vexp(y[1][0]);
  mfmav<false>(Sc[6][4], Kf[13][0], F[4][4]);
  Kf[14][3] = ld128(lds, aK[3] + 4096);
  mfmav<false>(Sc[6][0], Kf[13][1], F[5][0]);
  ls2[3][0] = y[0][0];
  mfmav<false>(Sc[6][1], Kf[13][1], F[5][1]);
  vexp(y[1][1]);
  mfmav<false>(Sc[6][2], Kf[13][1], F[5][2]);
  ls2[3][1] = y[0][1];
  dma<2048,true,false>(regb + 16384, vV[2], rV, 131072u);
  mfmav<false>(Sc[6][3], Kf[13][1], F[5][3]);
  vexp(y[1][2]);
  mfmav<false>(Sc[6][4], Kf[13][1], F[5][4]);
  pq[0][0] = vcvt(y[0][0], y[0][1]);
  vexp(y[1][3]);
  mfmav<false>(Sc[6][0], Kf[13][2], F[6][0]);
  vadd(ls2[3][0], y[0][2]);
  mfmav<false>(Sc[6][1], Kf[13][2], F[6][1]);
  vexp(y[1][4]);
  mfmav<false>(Sc[6][2], Kf[13][2], F[6][2]);
  vadd(ls2[3][1], y[0][3]);
  mfmav<false>(Sc[6][3], Kf[13][2], F[6][3]);
  vexp(y[1][5]);
  dma<3072,true,false>(regb + 16384, vV[3], rV, 131072u);
  mfmav<false>(Sc[6][4], Kf[13][2], F[6][4]);
  pq[0][1] = vcvt(y[0][2], y[0][3]);
  mfmav<false>(Sc[6][0], Kf[13][3], F[7][0]);
  vexp(y[1][6]);
  mfmav<false>(Sc[6][1], Kf[13][3], F[7][1]);
  vadd(ls2[3][0], y[0][4]);
  mfmav<false>(Sc[6][2], Kf[13][3], F[7][2]);
  vexp(y[1][7]);
  mfmav<false>(Sc[6][3], Kf[13][3], F[7][3]);
  vadd(ls2[3][1], y[0][5]);
  mfmav<false>(Sc[6][4], Kf[13][3], F[7][4]);
  pq[0][2] = vcvt(y[0][4], y[0][5]);
  vadd(ls2[3][0], y[0][6]);
  dma<0,true,true>(regb + 20480, vV[0], rV, 196608u);
  wait_lgkm0();
  mfmav<true>(Sc[7][0], Kf[14][0], F[0][0]);
  mfmav<true>(Sc[7][1], Kf[14][0], F[0][1]);
  wait_vm<13>();
  Kf[15][0] = ld128(lds, aK[0] + 8192);
  mfmav<true>(Sc[7][2], Kf[14][0], F[0][2]);
  Kf[15][1] = ld128(lds, aK[1] + 8192);
  vadd(ls2[3][1], y[0][7]);
  mfmav<true>(Sc[7][3], Kf[14][0], F[0][3]);
  Kf[15][2] = ld128(lds, aK[2] + 8192);
  mfmav<true>(Sc[7][4], Kf[14][0], F[0][4]);
  Kf[15][3] = ld128(lds, aK[3] + 8192);
  mx[0] = vmax3(mx[0], Sc[6][0][0], Sc[6][0][1]);
  mfmav<false>(Sc[7][0], Kf[14][1], F[1][0]);
  pq[0][3] = vcvt(y[0][6], y[0][7]);
  mx[1] = vmax3(mx[1], Sc[6][1][0], Sc[6][1][1]);
  mfmav<false>(Sc[7][1], Kf[14][1], F[1][1]);
  st128(lds, aP + 3072, pq[0]);
  dma<1024,true,false>(regb + 20480, vV[1], rV, 196608u);
  mfmav<false>(Sc[7][2], Kf[14][1], F[1][2]);
  mx[2] = vmax3(mx[2], Sc[6][2][0], Sc[6][2][1]);
  mfmav<false>(Sc[7][3], Kf[14][1], F[1][3]);
  ls2[4][0] = y[1][0];
  mx[3] = vmax3(mx[3], Sc[6][3][0], Sc[6][3][1]);
  mfmav<false>(Sc[7][4], Kf[14][1], F[1][4]);
  ls2[4][1] = y[1][1];
  mx[4] = vmax3(mx[4], Sc[6][4][0], Sc[6][4][1]);
  mfmav<false>(Sc[7][0], Kf[14][2], F[2][0]);
  pq[1][0] = vcvt(y[1][0], y[1][1]);
  mx[0] = vmax3(mx[0], Sc[6][0][2], Sc[6][0][3]);
  mfmav<false>(Sc[7][1], Kf[14][2], F[2][1]);
  vadd(ls2[4][0], y[1][2]);
  mfmav<false>(Sc[7][2], Kf[14][2], F[2][2]);
  mx[1] = vmax3(mx[1], Sc[6][1][2], Sc[6][1][3]);
  vadd(ls2[4][1], y[1][3]);
  dma<2048,true,false>(regb + 20480, vV[2], rV, 196608u);
  mfmav<false>(Sc[7][3], Kf[14][2], F[2][3]);
  mx[2] = vmax3(mx[2], Sc[6][2][2], Sc[6][2][3]);
  pq[1][1] = vcvt(y[1][2], y[1][3]);
  mfmav<false>(Sc[7][4], Kf[14][2], F[2][4]);
  mx[3] = vmax3(mx[3], Sc[6][3][2], Sc[6][3][3]);
  mfmav<false>(Sc[7][0], Kf[14][3], F[3][0]);
  vadd(ls2[4][0], y[1][4]);
  mx[4] = vmax3(mx[4], Sc[6][4][2], Sc[6][4][3]);
  mfmav<false>(Sc[7][1], Kf[14][3], F[3][1]);
  vadd(ls2[4][1], y[1][5]);
  pq[1][2] = vcvt(y[1][4], y[1][5]);
  mfmav<false>(Sc[7][2], Kf[14][3], F[3][2]);
  vadd(ls2[4][0], y[1][6]);
  vadd(ls2[4][1], y[1][7]);
  mfmav<false>(Sc[7][3], Kf[14][3], F[3][3]);
  pq[1][3] = vcvt(y[1][6], y[1][7]);
  dma<3072,true,false>(regb + 20480, vV[3], rV, 196608u);
  mfmav<false>(Sc[7][4], Kf[14][3], F[3][4]);
  st128(lds, aP + 4096, pq[1]);
  barrier();
  mfmav<false>(Sc[7][0], Kf[15][0], F[4][0]);
  mfmav<false>(Sc[7][1], Kf[15][0], F[4][1]);
  B[0][0] = ld128(lds, aB + 0);
  B[0][1] = ld128(lds, aB + 1024);
  mfmav<false>(Sc[7][2], Kf[15][0], F[4][2]);
  B[0][2] = ld128(lds, aB + 2048);
  mfmav<false>(Sc[7][3], Kf[15][0], F[4][3]);
  B[0][3] = ld128(lds, aB + 3072);
  B[0][4] = ld128(lds, aB + 4096);
  dma<0,true,true>(regb + 4096, vV[0], rV, 16384u);
  mfmav<false>(Sc[7][4], Kf[15][0], F[4][4]);
  wait_vm<13>();
  Vf[0][0] = ldtr(lds, aV[0] + 0, aV[0] + 2048);
  mfmav<false>(Sc[7][0], Kf[15][1], F[5][0]);
  Vf[0][1] = ldtr(lds, aV[1] + 0, aV[1] + 2048);
  mfmav<false>(Sc[7][1], Kf[15][1], F[5][1]);
  mfmav<false>(Sc[7][2], Kf[15][1], F[5][2]);
  Vf[0][2] = ldtr(lds, aV[2] + 0, aV[2] + 2048);
  mfmav<false>(Sc[7][3], Kf[15][1], F[5][3]);
  Vf[0][3] = ldtr(lds, aV[3] + 0, aV[3] + 2048);
  mfmav<false>(Sc[7][4], Kf[15][1], F[5][4]);
  mfmav<false>(Sc[7][0], Kf[15][2], F[6][0]);
  dma<1024,true,false>(regb + 4096, vV[1], rV, 16384u);
  mfmav<false>(Sc[7][1], Kf[15][2], F[6][1]);
  mfmav<false>(Sc[7][2], Kf[15][2], F[6][2]);
  mfmav<false>(Sc[7][3], Kf[15][2], F[6][3]);
  mfmav<false>(Sc[7][4], Kf[15][2], F[6][4]);
  mfmav<false>(Sc[7][0], Kf[15][3], F[7][0]);
  mfmav<false>(Sc[7][1], Kf[15][3], F[7][1]);
  dma<2048,true,false>(regb + 4096, vV[2], rV, 16384u);
  mfmav<false>(Sc[7][2], Kf[15][3], F[7][2]);
  mfmav<false>(Sc[7][3], Kf[15][3], F[7][3]);
  mfmav<false>(Sc[7][4], Kf[15][3], F[7][4]);
  wait_lgkm0();
  mfma<true>(acc[0][0], Vf[0][0], B[0][0]);
  mfma<true>(acc[0][1], Vf[0][0], B[0][1]);
  B[1][0] = ld128(lds, aB + 5120);
  B[1][1] = ld128(lds, aB + 6144);
  mfma<true>(acc[0][2], Vf[0][0], B[0][2]);
  B[1][2] = ld128(lds, aB + 7168);
  mx[0] = vmax3(mx[0], Sc[7][0][0], Sc[7][0][1]);
  mfma<true>(acc[0][3], Vf[0][0], B[0][3]);
  B[1][3] = ld128(lds, aB + 8192);
  B[1][4] = ld128(lds, aB + 9216);
  dma<3072,true,false>(regb + 4096, vV[3], rV, 16384u);
  mfma<true>(acc[0][4], Vf[0][0], B[0][4]);
  wait_vm<12>();
  Vf[1][0] = ldtr(lds, aV[0] + 12288, aV[0] + 14336);
  mfma<true>(acc[1][0], Vf[0][1], B[0][0]);
  Vf[1][1] = ldtr(lds, aV[1] + 12288, aV[1] + 14336);
  mfma<true>(acc[1][1], Vf[0][1], B[0][1]);
  mfma<true>(acc[1][2], Vf[0][1], B[0][2]);
  Vf[1][2] = ldtr(lds, aV[2] + 12288, aV[2] + 14336);
  mfma<true>(acc[1][3], Vf[0][1], B[0][3]);
  Vf[1][3] = ldtr(lds, aV[3] + 12288, aV[3] + 14336);
  mfma<true>(acc[1][4], Vf[0][1], B[0][4]);
  dma<0,true,true>(regb + 0, vV[0], rV, 81920u);
  mfma<true>(acc[2][0], Vf[0][2], B[0][0]);
  mx[1] = vmax3(mx[1], Sc[7][1][0], Sc[7][1][1]);
  mx[2] = vmax3(mx[2], Sc[7][2][0], Sc[7][2][1]);
  mfma<true>(acc[2][1], Vf[0][2], B[0][1]);
  mx[3] = vmax3(mx[3], Sc[7][3][0], Sc[7][3][1]);
  mfma<true>(acc[2][2], Vf[0][2], B[0][2]);
  mx[4] = vmax3(mx[4], Sc[7][4][0], Sc[7][4][1]);
  mx[0] = vmax3(mx[0], Sc[7][0][2], Sc[7][0][3]);
  mfma<true>(acc[2][3], Vf[0][2], B[0][3]);
  mx[1] = vmax3(mx[1], Sc[7][1][2], Sc[7][1][3]);
  mfma<true>(acc[2][4], Vf[0][2], B[0][4]);
  mx[2] = vmax3(mx[2], Sc[7][2][2], Sc[7][2][3]);
  mx[3] = vmax3(mx[3], Sc[7][3][2], Sc[7][3][3]);
  mfma<true>(acc[3][0], Vf[0][3], B[0][0]);
  mx[4] = vmax3(mx[4], Sc[7][4][2], Sc[7][4][3]);
  mfma<true>(acc[3][1], Vf[0][3], B[0][1]);
  SWAP16(0);
  dma<1024,true,false>(regb + 0, vV[1], rV, 81920u);
  mfma<true>(acc[3][2], Vf[0][3], B[0][2]);
  SWAP16(1);
  mfma<true>(acc[3][3], Vf[0][3], B[0][3]);
  mfma<true>(acc[3][4], Vf[0][3], B[0][4]);
  SWAP16(2);
  wait_lgkm0();
  mfma<false>(acc[0][0], Vf[1][0], B[1][0]);
  SWAP16(3);
  mfma<false>(acc[0][1], Vf[1][0], B[1][1]);
  B[0][0] = ld128(lds, aB + 10240);
  B[0][1] = ld128(lds, aB + 11264);
  mfma<false>(acc[0][2], Vf[1][0], B[1][2]);
  B[0][2] = ld128(lds, aB + 12288);
  dma<2048,true,false>(regb + 0, vV[2], rV, 81920u);
  mfma<false>(acc[0][3], Vf[1][0], B[1][3]);
  B[0][3] = ld128(lds, aB + 13312);
  B[0][4] = ld128(lds, aB + 14336);
  mfma<false>(acc[0][4], Vf[1][0], B[1][4]);
  wait_vm<11>();
  Vf[2][0] = ldtr(lds, aV[0] + 16384, aV[0] + 18432);
  mfma<false>(acc[1][0], Vf[1][1], B[1][0]);
  Vf[2][1] = ldtr(lds, aV[1] + 16384, aV[1] + 18432);
  mfma<false>(acc[1][1], Vf[1][1], B[1][1]);
  mfma<false>(acc[1][2], Vf[1][1], B[1][2]);
  Vf[2][2] = ldtr(lds, aV[2] + 16384, aV[2] + 18432);
  mfma<false>(acc[1][3], Vf[1][1], B[1][3]);
  Vf[2][3] = ldtr(lds, aV[3] + 16384, aV[3] + 18432);
  mfma<false>(acc[1][4], Vf[1][1], B[1][4]);
  dma<3072,true,false>(regb + 0, vV[3], rV, 81920u);
  mfma<false>(acc[2][0], Vf[1][2], B[1][0]);
  mfma<false>(acc[2][1], Vf[1][2], B[1][1]);
  SWAP16(4);
  mfma<false>(acc[2][2], Vf[1][2], B[1][2]);
  mx[0] = vmax3(t0_[0], t1_[0], t1_[0]);
  mfma<false>(acc[2][3], Vf[1][2], B[1][3]);
  mx[1] = vmax3(t0_[1], t1_[1], t1_[1]);
  mx[2] = vmax3(t0_[2], t1_[2], t1_[2]);
  mfma<false>(acc[2][4], Vf[1][2], B[1][4]);
  mx[3] = vmax3(t0_[3], t1_[3], t1_[3]);
  mfma<false>(acc[3][0], Vf[1][3], B[1][0]);
  mx[4] = vmax3(t0_[4], t1_[4], t1_[4]);
  dma<0,true,true>(regb + 8192, vV[0], rV, 147456u);
  mfma<false>(acc[3][1], Vf[1][3], B[1][1]);
  SWAP32(0);
  mfma<false>(acc[3][2], Vf[1][3], B[1][2]);
  SWAP32(1);
  mfma<false>(acc[3][3], Vf[1][3], B[1][3]);
  mfma<false>(acc[3][4], Vf[1][3], B[1][4]);
  SWAP32(2);
  wait_lgkm0();
  mfma<false>(acc[0][0], Vf[2][0], B[0][0]);
  SWAP32(3);
  mfma<false>(acc[0][1], Vf[2][0], B[0][1]);
  B[1][0] = ld128(lds, aB + 15360);
  B[1][1] = ld128(lds, aB + 16384);
  mfma<false>(acc[0][2], Vf[2][0], B[0][2]);
  B[1][2] = ld128(lds, aB + 17408);
  dma<1024,true,false>(regb + 8192, vV[1], rV, 147456u);
  mfma<false>(acc[0][3], Vf[2][0], B[0][3]);
  B[1][3] = ld128(lds, aB + 18432);
  B[1][4] = ld128(lds, aB + 19456);
  mfma<false>(acc[0][4], Vf[2][0], B[0][4]);
  wait_vm<10>();
  Vf[3][0] = ldtr(lds, aV[0] + 20480, aV[0] + 22528);
  dma<2048,true,false>(regb + 8192, vV[2], rV, 147456u);
  mfma<false>(acc[1][0], Vf[2][1], B[0][0]);
  Vf[3][1] = ldtr(lds, aV[1] + 20480, aV[1] + 22528);
  mfma<false>(acc[1][1], Vf[2][1], B[0][1]);
  mfma<false>(acc[1][2], Vf[2][1], B[0][2]);
  Vf[3][2] = ldtr(lds, aV[2] + 20480, aV[2] + 22528);
  mfma<false>(acc[1][3], Vf[2][1], B[0][3]);
  Vf[3][3] = ldtr(lds, aV[3] + 20480, aV[3] + 22528);
  mfma<false>(acc[1][4], Vf[2][1], B[0][4]);
  mfma<false>(acc[2][0], Vf[2][2], B[0][0]);
  mfma<false>(acc[2][1], Vf[2][2], B[0][1]);
  SWAP32(4);
  dma<3072,true,false>(regb + 8192, vV[3], rV, 147456u);
  mfma<false>(acc[2][2], Vf[2][2], B[0][2]);
  mx[0] = vmax3(t0_[0], t1_[0], t1_[0]);
  mfma<false>(acc[2][3], Vf[2][2], B[0][3]);
  mx[1] = vmax3(t0_[1], t1_[1], t1_[1]);
  mfma<false>(acc[2][4], Vf[2][2], B[0][4]);
  mx[2] = vmax3(t0_[2], t1_[2], t1_[2]);
  mfma<false>(acc[3][0], Vf[2][3], B[0][0]);
  mx[3] = vmax3(t0_[3], t1_[3], t1_[3]);
  mx[4] = vmax3(t0_[4], t1_[4], t1_[4]);
  mfma<false>(acc[3][1], Vf[2][3], B[0][1]);
  *(LAS float*)(lds + aS + ST_MAXB + 0) = mx[0];
  mfma<false>(acc[3][2], Vf[2][3], B[0][2]);
  *(LAS float*)(lds + aS + ST_MAXB + 256) = mx[1];
  dma<0,true,true>(regb + 12288, vV[0], rV, 212992u);
  mfma<false>(acc[3][3], Vf[2][3], B[0][3]);
  *(LAS float*)(lds + aS + ST_MAXB + 512) = mx[2];
  *(LAS float*)(lds + aS + ST_MAXB + 768) = mx[3];
  mfma<false>(acc[3][4], Vf[2][3], B[0][4]);
  *(LAS float*)(lds + aS + ST_MAXB + 1024) = mx[4];
  barrier();
  mfma<false>(acc[0][0], Vf[3][0], B[1][0]);
  mm[0] = *(LAS f32x4*)(lds + aSr + ST_MAXB + 0);
  mm[1] = *(LAS f32x4*)(lds + aSr + ST_MAXB + 256);
  mfma<false>(acc[0][1], Vf[3][0], B[1][1]);
  wait_vm<9>();
  Vf[4][0] = ldtr(lds, aV[0] + 4096, aV[0] + 6144);
  mm[2] = *(LAS f32x4*)(lds + aSr + ST_MAXB + 512);
  mm[3] = *(LAS f32x4*)(lds + aSr + ST_MAXB + 768);
  dma<1024,true,false>(regb + 12288, vV[1], rV, 212992u);
  mfma<false>(acc[0][2], Vf[3][0], B[1][2]);
  Vf[4][1] = ldtr(lds, aV[1] + 4096, aV[1] + 6144);
  mm[4] = *(LAS f32x4*)(lds + aSr + ST_MAXB + 1024);
  dma<2048,true,false>(regb + 12288, vV[2], rV, 212992u);
  mfma<false>(acc[0][3], Vf[3][0], B[1][3]);
  Vf[4][2] = ldtr(lds, aV[2] + 4096, aV[2] + 6144);
  mfma<false>(acc[0][4], Vf[3][0], B[1][4]);
  Vf[4][3] = ldtr(lds, aV[3] + 4096, aV[3] + 6144);
  mfma<false>(acc[1][0], Vf[3][1], B[1][0]);
  mfma<false>(acc[1][1], Vf[3][1], B[1][1]);
  mfma<false>(acc[1][2], Vf[3][1], B[1][2]);
  dma<3072,true,false>(regb + 12288, vV[3], rV, 212992u);
  mfma<false>(acc[1][3], Vf[3][1], B[1][3]);
  t0_[0] = vmax3(mm[0][0], mm[0][1], mm[0][2]);
  t0_[1] = vmax3(mm[1][0], mm[1][1], mm[1][2]);
  t0_[2] = vmax3(mm[2][0], mm[2][1], mm[2][2]);
  mfma<false>(acc[1][4], Vf[3][1], B[1][4]);
  t0_[3] = vmax3(mm[3][0], mm[3][1], mm[3][2]);
  t0_[4] = vmax3(mm[4][0], mm[4][1], mm[4][2]);
  mfma<false>(acc[2][0], Vf[3][2], B[1][0]);
  t0_[0] = vmax3(t0_[0], mm[0][3], mA[0]);
  t0_[1] = vmax3(t0_[1], mm[1][3], mA[1]);
  t0_[2] = vmax3(t0_[2], mm[2][3], mA[2]);
  mfma<false>(acc[2][1], Vf[3][2], B[1][1]);
  t0_[3] = vmax3(t0_[3], mm[3][3], mA[3]);
  t0_[4] = vmax3(t0_[4], mm[4][3], mA[4]);
  mfma<false>(acc[2][2], Vf[3][2], B[1][2]);
  nmB[0] = vmul(t0_[0], ncs);
  nmB[1] = vmul(t0_[1], ncs);
  nmB[2] = vmul(t0_[2], ncs);
  mfma<false>(acc[2][3], Vf[3][2], B[1][3]);
  nmB[3] = vmul(t0_[3], ncs);
  nmB[4] = vmul(t0_[4], ncs);
  dma<0,true,true>(regb + 16384, vV[0], rV, 32768u);
  mfma<false>(acc[2][4], Vf[3][2], B[1][4]);
  fa[0] = vfma(mA[0], nmB[0]);
  fa[1] = vfma(mA[1], nmB[1]);
  fa[2] = vfma(mA[2], nmB[2]);
  mfma<false>(acc[3][0], Vf[3][3], B[1][0]);
  fa[3] = vfma(mA[3], nmB[3]);
  fa[4] = vfma(mA[4], nmB[4]);
  mfma<false>(acc[3][1], Vf[3][3], B[1][1]);
  vexp(fa[0]);
  mfma<false>(acc[3][2], Vf[3][3], B[1][2]);
  vexp(fa[1]);
  vexp(fa[2]);
  mfma<false>(acc[3][3], Vf[3][3], B[1][3]);
  vexp(fa[3]);
  mfma<false>(acc[3][4], Vf[3][3], B[1][4]);
  vexp(fa[4]);
  dma<1024,true,false>(regb + 16384, vV[1], rV, 32768u);
  mfma_pad();
  SM_PREP();
  dma<2048,true,false>(regb + 16384, vV[2], rV, 32768u);
  yp[0][0] = vpkfma(LOHI(Sc[2][0], 0), nm2[0]);
  yp[0][1] = vpkfma(LOHI(Sc[2][0], 1), nm2[0]);
  yp[0][2] = vpkfma(LOHI(Sc[3][0], 0), nm2[0]);
  yp[0][3] = vpkfma(LOHI(Sc[3][0], 1), nm2[0]);
  vexp2(yp[0][0], 0);
  yp[1][0] = vpkfma(LOHI(Sc[2][1], 0), nm2[1]);
  vexp2(yp[0][0], 1);
  yp[1][1] = vpkfma(LOHI(Sc[2][1], 1), nm2[1]);
  vexp2(yp[0][1], 0);
  yp[1][2] = vpkfma(LOHI(Sc[3][1], 0), nm2[1]);
  vexp2(yp[0][1], 1);
  yp[1][3] = vpkfma(LOHI(Sc[3][1], 1), nm2[1]);
  vexp2(yp[0][2], 0);
  vexp2(yp[0][2], 1);
  vexp2(yp[0][3], 0);
  vexp2(yp[0][3], 1);
  vexp2(yp[1][0], 0);
  pq[0][0] = vcvt(yp[0][0][0], yp[0][0][1]);
  vexp2(yp[1][0], 1);
  vpkadd(lp[0], yp[0][0]);
  dma<3072,true,false>(regb + 16384, vV[3], rV, 32768u);
  vexp2(yp[1][1], 0);
  pq[0][1] = vcvt(yp[0][1][0], yp[0][1][1]);
  vexp2(yp[1][1], 1);
  vpkadd(lp[0], yp[0][1]);
  vexp2(yp[1][2], 0);
  pq[0][2] = vcvt(yp[0][2][0], yp[0][2][1]);
  vexp2(yp[1][2], 1);
  vpkadd(lp[0], yp[0][2]);
  vexp2(yp[1][3], 0);
  pq[0][3] = vcvt(yp[0][3][0], yp[0][3][1]);
  dma<0,true,true>(regb + 20480, vV[0], rV, 98304u);
  vexp2(yp[1][3], 1);
  vpkadd(lp[0], yp[0][3]);
  st128(lds, aP + 20480, pq[0]);
  yp[2][0] = vpkfma(LOHI(Sc[2][2], 0), nm2[2]);
  yp[2][1] = vpkfma(LOHI(Sc[2][2], 1), nm2[2]);
  yp[2][2] = vpkfma(LOHI(Sc[3][2], 0), nm2[2]);
  yp[2][3] = vpkfma(LOHI(Sc[3][2], 1), nm2[2]);
  vexp2(yp[2][0], 0);
  pq[1][0] = vcvt(yp[1][0][0], yp[1][0][1]);
  vexp2(yp[2][0], 1);
  vpkadd(lp[1], yp[1][0]);
  vexp2(yp[2][1], 0);
  pq[1][1] = vcvt(yp[1][1][0], yp[1][1][1]);
  vexp2(yp[2][1], 1);
  vpkadd(lp[1], yp[1][1]);
  vexp2(yp[2][2], 0);
  pq[1][2] = vcvt(yp[1][2][0], yp[1][2][1]);
  vexp2(yp[2][2], 1);
  vpkadd(lp[1], yp[1][2]);
  vexp2(yp[2][3], 0);
  dma<1024,true,false>(regb + 20480, vV[1], rV, 98304u);
  pq[1][3] = vcvt(yp[1][3][0], yp[1][3][1]);
  vexp2(yp[2][3], 1);
  vpkadd(lp[1], yp[1][3]);
  st128(lds, aP + 21504, pq[1]);
  yp[0][0] = vpkfma(LOHI(Sc[2][3], 0), nm2[3]);
  yp[0][1] = vpkfma(LOHI(Sc[2][3], 1), nm2[3]);
  yp[0][2] = vpkfma(LOHI(Sc[3][3], 0), nm2[3]);
  yp[0][3] = vpkfma(LOHI(Sc[3][3], 1), nm2[3]);
  vexp2(yp[0][0], 0);
  pq[2][0] = vcvt(yp[2][0][0], yp[2][0][1]);
  vexp2(yp[0][0], 1);
  vpkadd(lp[2], yp[2][0]);
  vexp2(yp[0][1], 0);
  pq[2][1] = vcvt(yp[2][1][0], yp[2][1][1]);
  vexp2(yp[0][1], 1);
  vpkadd(lp[2], yp[2][1]);
  vexp2(yp[0][2], 0);
  pq[2][2] = vcvt(yp[2][2][0], yp[2][2][1]);
  vexp2(yp[0][2], 1);
  vpkadd(lp[2], yp[2][2]);
  dma<2048,true,false>(regb + 20480, vV[2], rV, 98304u);
  vexp2(yp[0][3], 0);
  pq[2][3] = vcvt(yp[2][3][0], yp[2][3][1]);
  vexp2(yp[0][3], 1);
  vpkadd(lp[2], yp[2][3]);
  st128(lds, aP + 22528, pq[2]);
  yp[1][0] = vpkfma(LOHI(Sc[2][4], 0), nm2[4]);
  yp[1][1] = vpkfma(LOHI(Sc[2][4], 1), nm2[4]);
  yp[1][2] = vpkfma(LOHI(Sc[3][4], 0), nm2[4]);
  yp[1][3] = vpkfma(LOHI(Sc[3][4], 1), nm2[4]);
  vexp2(yp[1][0], 0);
  pq[0][0] = vcvt(yp[0][0][0], yp[0][0][1]);
  vexp2(yp[1][0], 1);
  vpkadd(lp[3], yp[0][0]);
  vexp2(yp[1][1], 0);
  pq[0][1] = vcvt(yp[0][1][0], yp[0][1][1]);
  vexp2(yp[1][1], 1);
  vpkadd(lp[3], yp[0][1]);
  vexp2(yp[1][2], 0);
  pq[0][2] = vcvt(yp[0][2][0], yp[0][2][1]);
  vexp2(yp[1][2], 1);
  dma<3072,true,false>(regb + 20480, vV[3], rV, 98304u);
  vpkadd(lp[3], yp[0][2]);
  vexp2(yp[1][3], 0);
  pq[0][3] = vcvt(yp[0][3][0], yp[0][3][1]);
  vexp2(yp[1][3], 1);
  vpkadd(lp[3], yp[0][3]);
  st128(lds, aP + 23552, pq[0]);
  yp[2][0] = vpkfma(LOHI(Sc[4][0], 0), nm2[0]);
  yp[2][1] = vpkfma(LOHI(Sc[4][0], 1), nm2[0]);
  yp[2][2] = vpkfma(LOHI(Sc[5][0], 0), nm2[0]);
  yp[2][3] = vpkfma(LOHI(Sc[5][0], 1), nm2[0]);
  SETUP3();
  vexp2(yp[2][0], 0);
  WO_TOUCH();
  pq[1][0] = vcvt(yp[1][0][0], yp[1][0][1]);
  vexp2(yp[2][0], 1);
  vpkadd(lp[4], yp[1][0]);
  vexp2(yp[2][1], 0);
  pq[1][1] = vcvt(yp[1][1][0], yp[1][1][1]);
  vexp2(yp[2][1], 1);
  vpkadd(lp[4], yp[1][1]);
  vexp2(yp[2][2], 0);
  pq[1][2] = vcvt(yp[1][2][0], yp[1][2][1]);
  vexp2(yp[2][2], 1);
  vpkadd(lp[4], yp[1][2]);
  vexp2(yp[2][3], 0);
  pq[1][3] = vcvt(yp[1][3][0], yp[1][3][1]);
  vexp2(yp[2][3], 1);
  vpkadd(lp[4], yp[1][3]);
  st128(lds, aP + 24576, pq[1]);
  yp[0][0] = vpkfma(LOHI(Sc[4][1], 0), nm2[1]);
  yp[0][1] = vpkfma(LOHI(Sc[4][1], 1), nm2[1]);
  yp[0][2] = vpkfma(LOHI(Sc[5][1], 0), nm2[1]);
  dma<0,true,true>(regb + 24576, vV[0], rV, 163840u);
  yp[0][3] = vpkfma(LOHI(Sc[5][1], 1), nm2[1]);
  vexp2(yp[0][0], 0);
  pq[2][0] = vcvt(yp[2][0][0], yp[2][0][1]);
  vexp2(yp[0][0], 1);
  vpkadd(lp[0], yp[2][0]);
  vexp2(yp[0][1], 0);
  pq[2][1] = vcvt(yp[2][1][0], yp[2][1][1]);
  vexp2(yp[0][1], 1);
  vpkadd(lp[0], yp[2][1]);
  vexp2(yp[0][2], 0);
  pq[2][2] = vcvt(yp[2][2][0], yp[2][2][1]);
  vexp2(yp[0][2], 1);
  vpkadd(lp[0], yp[2][2]);
  vexp2(yp[0][3], 0);
  pq[2][3] = vcvt(yp[2][3][0], yp[2][3][1]);
  vexp2(yp[0][3], 1);
  vpkadd(lp[0], yp[2][3]);
  st128(lds, aP + 0, pq[2]);
  yp[1][0] = vpkfma(LOHI(Sc[4][2], 0), nm2[2]);
  yp[1][1] = vpkfma(LOHI(Sc[4][2], 1), nm2[2]);
  dma<1024,true,false>(regb + 24576, vV[1], rV, 163840u);
  yp[1][2] = vpkfma(LOHI(Sc[5][2], 0), nm2[2]);
  yp[1][3] = vpkfma(LOHI(Sc[5][2], 1), nm2[2]);
  vexp2(yp[1][0], 0);
  pq[0][0] = vcvt(yp[0][0][0], yp[0][0][1]);
  vexp2(yp[1][0], 1);
  vpkadd(lp[1], yp[0][0]);
  vexp2(yp[1][1], 0);
  pq[0][1] = vcvt(yp[0][1][0], yp[0][1][1]);
  vexp2(yp[1][1], 1);
  vpkadd(lp[1], yp[0][1]);
  vexp2(yp[1][2], 0);
  pq[0][2] = vcvt(yp[0][2][0], yp[0][2][1]);
  vexp2(yp[1][2], 1);
  vpkadd(lp[1], yp[0][2]);
  vexp2(yp[1][3], 0);
  pq[0][3] = vcvt(yp[0][3][0], yp[0][3][1]);
  vexp2(yp[1][3], 1);
  vpkadd(lp[1], yp[0][3]);
  st128(lds, aP + 1024, pq[0]);
  yp[2][0] = vpkfma(LOHI(Sc[4][3], 0), nm2[3]);
  dma<2048,true,false>(regb + 24576, vV[2], rV, 163840u);
  yp[2][1] = vpkfma(LOHI(Sc[4][3], 1), nm2[3]);
  yp[2][2] = vpkfma(LOHI(Sc[5][3], 0), nm2[3]);
  yp[2][3] = vpkfma(LOHI(Sc[5][3], 1), nm2[3]);
  vexp2(yp[2][0], 0);
  pq[1][0] = vcvt(yp[1][0][0], yp[1][0][1]);
  vexp2(yp[2][0], 1);
  vpkadd(lp[2], yp[1][0]);
  vexp2(yp[2][1], 0);
  pq[1][1] = vcvt(yp[1][1][0], yp[1][1][1]);
  vexp2(yp[2][1], 1);
  vpkadd(lp[2], yp[1][1]);
  vexp2(yp[2][2], 0);
  pq[1][2] = vcvt(yp[1][2][0], yp[1][2][1]);
  vexp2(yp[2][2], 1);
  vpkadd(lp[2], yp[1][2]);
  vexp2(yp[2][3], 0);
  pq[1][3] = vcvt(yp[1][3][0], yp[1][3][1]);
  vexp2(yp[2][3], 1);
  vpkadd(lp[2], yp[1][3]);
  st128(lds, aP + 2048, pq[1]);
  dma<3072,true,false>(regb + 24576, vV[3], rV, 163840u);
  yp[0][0] = vpkfma(LOHI(Sc[4][4], 0), nm2[4]);
  yp[0][1] = vpkfma(LOHI(Sc[4][4], 1), nm2[4]);
  yp[0][2] = vpkfma(LOHI(Sc[5][4], 0), nm2[4]);
  yp[0][3] = vpkfma(LOHI(Sc[5][4], 1), nm2[4]);
  vexp2(yp[0][0], 0);
  pq[2][0] = vcvt(yp[2][0][0], yp[2][0][1]);
  vexp2(yp[0][0], 1);
  vpkadd(lp[3], yp[2][0]);
  vexp2(yp[0][1], 0);
  pq[2][1] = vcvt(yp[2][1][0], yp[2][1][1]);
  vexp2(yp[0][1], 1);
  vpkadd(lp[3], yp[2][1]);
  vexp2(yp[0][2], 0);
  pq[2][2] = vcvt(yp[2][2][0], yp[2][2][1]);
  vexp2(yp[0][2], 1);
  vpkadd(lp[3], yp[2][2]);
  vexp2(yp[0][3], 0);
  pq[2][3] = vcvt(yp[2][3][0], yp[2][3][1]);
  vexp2(yp[0][3], 1);
  vpkadd(lp[3], yp[2][3]);
  st128(lds, aP + 3072, pq[2]);
  yp[1][0] = vpkfma(LOHI(Sc[6][0], 0), nm2[0]);
  yp[1][1] = vpkfma(LOHI(Sc[6][0], 1), nm2[0]);
  yp[1][2] = vpkfma(LOHI(Sc[7][0], 0), nm2[0]);
  yp[1][3] = vpkfma(LOHI(Sc[7][0], 1), nm2[0]);
  vexp2(yp[1][0], 0);
  pq[0][0] = vcvt(yp[0][0][0], yp[0][0][1]);
  vexp2(yp[1][0], 1);
  vpkadd(lp[4], yp[0][0]);
  vexp2(yp[1][1], 0);
  pq[0][1] = vcvt(yp[0][1][0], yp[0][1][1]);
  vexp2(yp[1][1], 1);
  vpkadd(lp[4], yp[0][1]);
  vexp2(yp[1][2], 0);
  pq[0][2] = vcvt(yp[0][2][0], yp[0][2][1]);
  vexp2(yp[1][2], 1);
  vpkadd(lp[4], yp[0][2]);
  vexp2(yp[1][3], 0);
  pq[0][3] = vcvt(yp[0][3][0], yp[0][3][1]);
  vexp2(yp[1][3], 1);
  vpkadd(lp[4], yp[0][3]);
  st128(lds, aP + 4096, pq[0]);
  yp[2][0] = vpkfma(LOHI(Sc[6][1], 0), nm2[1]);
  yp[2][1] = vpkfma(LOHI(Sc[6][1], 1), nm2[1]);
  yp[2][2] = vpkfma(LOHI(Sc[7][1], 0), nm2[1]);
  yp[2][3] = vpkfma(LOHI(Sc[7][1], 1), nm2[1]);
  vexp2(yp[2][0], 0);
  P3[0][0] = vcvt(yp[1][0][0], yp[1][0][1]);
  vexp2(yp[2][0], 1);
  vpkadd(lp[0], yp[1][0]);
  vexp2(yp[2][1], 0);
  P3[0][1] = vcvt(yp[1][1][0], yp[1][1][1]);
  vexp2(yp[2][1], 1);
  vpkadd(lp[0], yp[1][1]);
  vexp2(yp[2][2], 0);
  P3[0][2] = vcvt(yp[1][2][0], yp[1][2][1]);
  vexp2(yp[2][2], 1);
  vpkadd(lp[0], yp[1][2]);
  vexp2(yp[2][3], 0);
  P3[0][3] = vcvt(yp[1][3][0], yp[1][3][1]);
  vexp2(yp[2][3], 1);
  vpkadd(lp[0], yp[1][3]);
  yp[0][0] = vpkfma(LOHI(Sc[6][2], 0), nm2[2]);
  yp[0][1] = vpkfma(LOHI(Sc[6][2], 1), nm2[2]);
  yp[0][2] = vpkfma(LOHI(Sc[7][2], 0), nm2[2]);
  yp[0][3] = vpkfma(LOHI(Sc[7][2], 1), nm2[2]);
  vexp2(yp[0][0], 0);
  P3[1][0] = vcvt(yp[2][0][0], yp[2][0][1]);
  vexp2(yp[0][0], 1);
  vpkadd(lp[1], yp[2][0]);
  vexp2(yp[0][1], 0);
  P3[1][1] = vcvt(yp[2][1][0], yp[2][1][1]);
  vexp2(yp[0][1], 1);
  vpkadd(lp[1], yp[2][1]);
  vexp2(yp[0][2], 0);
  P3[1][2] = vcvt(yp[2][2][0], yp[2][2][1]);
  vexp2(yp[0][2], 1);
  vpkadd(lp[1], yp[2][2]);
  vexp2(yp[0][3], 0);
  P3[1][3] = vcvt(yp[2][3][0], yp[2][3][1]);
  vexp2(yp[0][3], 1);
  vpkadd(lp[1], yp[2][3]);
  yp[1][0] = vpkfma(LOHI(Sc[6][3], 0), nm2[3]);
  yp[1][1] = vpkfma(LOHI(Sc[6][3], 1), nm2[3]);
  yp[1][2] = vpkfma(LOHI(Sc[7][3], 0), nm2[3]);
  yp[1][3] = vpkfma(LOHI(Sc[7][3], 1), nm2[3]);
  vexp2(yp[1][0], 0);
  P3[2][0] = vcvt(yp[0][0][0], yp[0][0][1]);
  vexp2(yp[1][0], 1);
  vpkadd(lp[2], yp[0][0]);
  vexp2(yp[1][1], 0);
  P3[2][1] = vcvt(yp[0][1][0], yp[0][1][1]);
  vexp2(yp[1][1], 1);
  vpkadd(lp[2], yp[0][1]);
  vexp2(yp[1][2], 0);
  P3[2][2] = vcvt(yp[0][2][0], yp[0][2][1]);
  vexp2(yp[1][2], 1);
  vpkadd(lp[2], yp[0][2]);
  vexp2(yp[1][3], 0);
  P3[2][3] = vcvt(yp[0][3][0], yp[0][3][1]);
  vexp2(yp[1][3], 1);
  vpkadd(lp[2], yp[0][3]);
  yp[2][0] = vpkfma(LOHI(Sc[6][4], 0), nm2[4]);
  yp[2][1] = vpkfma(LOHI(Sc[6][4], 1), nm2[4]);
  yp[2][2] = vpkfma(LOHI(Sc[7][4], 0), nm2[4]);
  yp[2][3] = vpkfma(LOHI(Sc[7][4], 1), nm2[4]);
  vexp2(yp[2][0], 0);
  P3[3][0] = vcvt(yp[1][0][0], yp[1][0][1]);
  vexp2(yp[2][0], 1);
  vpkadd(lp[3], yp[1][0]);
  vexp2(yp[2][1], 0);
  P3[3][1] = vcvt(yp[1][1][0], yp[1][1][1]);
  vexp2(yp[2][1], 1);
  vpkadd(lp[3], yp[1][1]);
  vexp2(yp[2][2], 0);
  P3[3][2] = vcvt(yp[1][2][0], yp[1][2][1]);
  vexp2(yp[2][2], 1);
  vpkadd(lp[3], yp[1][2]);
  vexp2(yp[2][3], 0);
  P3[3][3] = vcvt(yp[1][3][0], yp[1][3][1]);
  vexp2(yp[2][3], 1);
  vpkadd(lp[3], yp[1][3]);
  P3[4][0] = vcvt(yp[2][0][0], yp[2][0][1]);
  vpkadd(lp[4], yp[2][0]);
  P3[4][1] = vcvt(yp[2][1][0], yp[2][1][1]);
  vpkadd(lp[4], yp[2][1]);
  P3[4][2] = vcvt(yp[2][2][0], yp[2][2][1]);
  vpkadd(lp[4], yp[2][2]);
  P3[4][3] = vcvt(yp[2][3][0], yp[2][3][1]);
  vpkadd(lp[4], yp[2][3]);
  barrier();
  B[0][0] = ld128(lds, aB + 20480);
  B[0][1] = ld128(lds, aB + 21504);
  B[0][2] = ld128(lds, aB + 22528);
  B[0][3] = ld128(lds, aB + 23552);
  B[0][4] = ld128(lds, aB + 24576);
  dma<0,true,true>(regb + 4096, vV[0], rV, 229376u);
  dma<1024,true,false>(regb + 4096, vV[1], rV, 229376u);
  mfmab<false>(acc[0][0], Vf[4][0], B[0][0]);
  dma<2048,true,false>(regb + 4096, vV[2], rV, 229376u);
  dma<3072,true,false>(regb + 4096, vV[3], rV, 229376u);
  mfmab<false>(acc[0][1], Vf[4][0], B[0][1]);
  B[1][0] = ld128(lds, aB + 25600);
  B[1][1] = ld128(lds, aB + 26624);
  mfmab<false>(acc[0][2], Vf[4][0], B[0][2]);
  B[1][2] = ld128(lds, aB + 27648);
  B[1][3] = ld128(lds, aB + 28672);
  mfmab<false>(acc[0][3], Vf[4][0], B[0][3]);
  B[1][4] = ld128(lds, aB + 29696);
  wait_vm<25>();
  Vf[5][0] = ldtr(lds, aV[0] + 0, aV[0] + 2048);
  mfmab<false>(acc[0][4], Vf[4][0], B[0][4]);
  Vf[5][1] = ldtr(lds, aV[1] + 0, aV[1] + 2048);
  mfmab<false>(acc[1][0], Vf[4][1], B[0][0]);
  Vf[5][2] = ldtr(lds, aV[2] + 0, aV[2] + 2048);
  mfmab<false>(acc[1][1], Vf[4][1], B[0][1]);
  Vf[5][3] = ldtr(lds, aV[3] + 0, aV[3] + 2048);
  mfmab<false>(acc[1][2], Vf[4][1], B[0][2]);
  wait_vm<21>();
  Vf[6][0] = ldtr(lds, aV[0] + 8192, aV[0] + 10240);
  mfmab<false>(acc[1][3], Vf[4][1], B[0][3]);
  Vf[6][1] = ldtr(lds, aV[1] + 8192, aV[1] + 10240);
  mfmab<false>(acc[1][4], Vf[4][1], B[0][4]);
  Vf[6][2] = ldtr(lds, aV[2] + 8192, aV[2] + 10240);
  mfmab<false>(acc[2][0], Vf[4][2], B[0][0]);
  Vf[6][3] = ldtr(lds, aV[3] + 8192, aV[3] + 10240);
  mfmab<false>(acc[2][1], Vf[4][2], B[0][1]);
  wait_vm<17>();
  Vf[7][0] = ldtr(lds, aV[0] + 12288, aV[0] + 14336);
  mfmab<false>(acc[2][2], Vf[4][2], B[0][2]);
  Vf[7][1] = ldtr(lds, aV[1] + 12288, aV[1] + 14336);
  mfmab<false>(acc[2][3], Vf[4][2], B[0][3]);
  Vf[7][2] = ldtr(lds, aV[2] + 12288, aV[2] + 14336);
  mfmab<false>(acc[2][4], Vf[4][2], B[0][4]);
  Vf[7][3] = ldtr(lds, aV[3] + 12288, aV[3] + 14336);
  mfmab<false>(acc[3][0], Vf[4][3], B[0][0]);
  mfmab<false>(acc[3][1], Vf[4][3], B[0][1]);
  mfmab<false>(acc[3][2], Vf[4][3], B[0][2]);
  mfmab<false>(acc[3][3], Vf[4][3], B[0][3]);
  mfmab<false>(acc[3][4], Vf[4][3], B[0][4]);
  wait_lgkm0();
  mfmab<false>(acc[0][0], Vf[5][0], B[1][0]);
  mx[0] = vaddr(lp[0][0], lp[0][1]);
  mx[1] = vaddr(lp[1][0], lp[1][1]);
  dma<0,true,true>(regb + 0, vV[0], rV, 49152u);
  mfmab<false>(acc[0][1], Vf[5][0], B[1][1]);
  B[0][0] = ld128(lds, aB + 30720);
  B[0][1] = ld128(lds, aB + 31744);
  dma<1024,true,false>(regb + 0, vV[1], rV, 49152u);
  mfmab<false>(acc[0][2], Vf[5][0], B[1][2]);
  B[0][2] = ld128(lds, aB + 32768);
  dma<2048,true,false>(regb + 0, vV[2], rV, 49152u);
  dma<3072,true,false>(regb + 0, vV[3], rV, 49152u);
  mfmab<false>(acc[0][3], Vf[5][0], B[1][3]);
  B[0][3] = ld128(lds, aB + 33792);
  B[0][4] = ld128(lds, aB + 34816);
  dma<0,true,true>(regb + 8192, vV[0], rV, 114688u);
  mfmab<false>(acc[0][4], Vf[5][0], B[1][4]);
  wait_vm<18>();
  Vf[8][0] = ldtr(lds, aV[0] + 16384, aV[0] + 18432);
  dma<1024,true,false>(regb + 8192, vV[1], rV, 114688u);
  mfmab<false>(acc[1][0], Vf[5][1], B[1][0]);
  Vf[8][1] = ldtr(lds, aV[1] + 16384, aV[1] + 18432);
  dma<2048,true,false>(regb + 8192, vV[2], rV, 114688u);
  mfmab<false>(acc[1][1], Vf[5][1], B[1][1]);
  mx[2] = vaddr(lp[2][0], lp[2][1]);
  mfmab<false>(acc[1][2], Vf[5][1], B[1][2]);
  Vf[8][2] = ldtr(lds, aV[2] + 16384, aV[2] + 18432);
  mfmab<false>(acc[1][3], Vf[5][1], B[1][3]);
  Vf[8][3] = ldtr(lds, aV[3] + 16384, aV[3] + 18432);
  mfmab<false>(acc[1][4], Vf[5][1], B[1][4]);
  mfmab<false>(acc[2][0], Vf[5][2], B[1][0]);
  mx[3] = vaddr(lp[3][0], lp[3][1]);
  mx[4] = vaddr(lp[4][0], lp[4][1]);
  mfmab<false>(acc[2][1], Vf[5][2], B[1][1]);
  SWAP16(0);
  mfmab<false>(acc[2][2], Vf[5][2], B[1][2]);
  SWAP16(1);
  mfmab<false>(acc[2][3], Vf[5][2], B[1][3]);
  SWAP16(2);
  mfmab<false>(acc[2][4], Vf[5][2], B[1][4]);
  mfmab<false>(acc[3][0], Vf[5][3], B[1][0]);
  SWAP16(3);
  mfmab<false>(acc[3][1], Vf[5][3], B[1][1]);
  SWAP16(4);
  mfmab<false>(acc[3][2], Vf[5][3], B[1][2]);
  mx[0] = vaddr(t0_[0], t1_[0]);
  mx[1] = vaddr(t0_[1], t1_[1]);
  mfmab<false>(acc[3][3], Vf[5][3], B[1][3]);
  mx[2] = vaddr(t0_[2], t1_[2]);
  mfmab<false>(acc[3][4], Vf[5][3], B[1][4]);
  mx[3] = vaddr(t0_[3], t1_[3]);
  mx[4] = vaddr(t0_[4], t1_[4]);
  wait_lgkm0();
  mfmab<false>(acc[0][0], Vf[6][0], B[0][0]);
  SWAP32(0);
  mfmab<false>(acc[0][1], Vf[6][0], B[0][1]);
  B[1][0] = ld128(lds, aB + 35840);
  B[1][1] = ld128(lds, aB + 36864);
  mfmab<false>(acc[0][2], Vf[6][0], B[0][2]);
  B[1][2] = ld128(lds, aB + 37888);
  B[1][3] = ld128(lds, aB + 38912);
  mfmab<false>(acc[0][3], Vf[6][0], B[0][3]);
  B[1][4] = ld128(lds, aB + 39936);
  SWAP32(1);
  mfmab<false>(acc[0][4], Vf[6][0], B[0][4]);
  wait_vm<16>();
  Vf[9][0] = ldtr(lds, aV[0] + 20480, aV[0] + 22528);
  mfmab<false>(acc[1][0], Vf[6][1], B[0][0]);
  Vf[9][1] = ldtr(lds, aV[1] + 20480, aV[1] + 22528);
  dma<3072,true,false>(regb + 8192, vV[3], rV, 114688u);
  mfmab<false>(acc[1][1], Vf[6][1], B[0][1]);
  Vf[9][2] = ldtr(lds, aV[2] + 20480, aV[2] + 22528);
  mfmab<false>(acc[1][2], Vf[6][1], B[0][2]);
  Vf[9][3] = ldtr(lds, aV[3] + 20480, aV[3] + 22528);
  mfmab<false>(acc[1][3], Vf[6][1], B[0][3]);
  SWAP32(2);
  mfmab<false>(acc[1][4], Vf[6][1], B[0][4]);
  wait_vm<12>();
  Vf[10][0] = ldtr(lds, aV[0] + 24576, aV[0] + 26624);
  mfmab<false>(acc[2][0], Vf[6][2], B[0][0]);
  Vf[10][1] = ldtr(lds, aV[1] + 24576, aV[1] + 26624);
  mfmab<false>(acc[2][1], Vf[6][2], B[0][1]);
  Vf[10][2] = ldtr(lds, aV[2] + 24576, aV[2] + 26624);
  dma<0,true,true>(regb + 12288, vV[0], rV, 180224u);
  mfmab<false>(acc[2][2], Vf[6][2], B[0][2]);
  Vf[10][3] = ldtr(lds, aV[3] + 24576, aV[3] + 26624);
  mfmab<false>(acc[2][3], Vf[6][2], B[0][3]);
  SWAP32(3);
  mfmab<false>(acc[2][4], Vf[6][2], B[0][4]);
  SWAP32(4);
  mfmab<false>(acc[3][0], Vf[6][3], B[0][0]);
  mx[0] = vaddr(t0_[0], t1_[0]);
  mx[1] = vaddr(t0_[1], t1_[1]);
  mfmab<false>(acc[3][1], Vf[6][3], B[0][1]);
  mx[2] = vaddr(t0_[2], t1_[2]);
  mx[3] = vaddr(t0_[3], t1_[3]);
  mfmab<false>(acc[3][2], Vf[6][3], B[0][2]);
  mx[4] = vaddr(t0_[4], t1_[4]);
  *(LAS float*)(lds + aS + ST_SUM + 0) = mx[0];
  mfmab<false>(acc[3][3], Vf[6][3], B[0][3]);
  *(LAS float*)(lds + aS + ST_SUM + 256) = mx[1];
  *(LAS float*)(lds + aS + ST_SUM + 512) = mx[2];
  dma<1024,true,false>(regb + 12288, vV[1], rV, 180224u);
  mfmab<false>(acc[3][4], Vf[6][3], B[0][4]);
  *(LAS float*)(lds + aS + ST_SUM + 768) = mx[3];
  *(LAS float*)(lds + aS + ST_SUM + 1024) = mx[4];
  barrier();
  mfmab<false>(acc[0][0], Vf[7][0], B[1][0]);
  mfmab<false>(acc[0][1], Vf[7][0], B[1][1]);
  B[0][0] = ld128(lds, aB + 0);
  mfmab<false>(acc[0][2], Vf[7][0], B[1][2]);
  B[0][1] = ld128(lds, aB + 1024);
  dma<2048,true,false>(regb + 12288, vV[2], rV, 180224u);
  mfmab<false>(acc[0][3], Vf[7][0], B[1][3]);
  B[0][2] = ld128(lds, aB + 2048);
  mfmab<false>(acc[0][4], Vf[7][0], B[1][4]);
  B[0][3] = ld128(lds, aB + 3072);
  mfmab<false>(acc[1][0], Vf[7][1], B[1][0]);
  B[0][4] = ld128(lds, aB + 4096);
  mfmab<false>(acc[1][1], Vf[7][1], B[1][1]);
  mfmab<false>(acc[1][2], Vf[7][1], B[1][2]);
  mfmab<false>(acc[1][3], Vf[7][1], B[1][3]);
  mfmab<false>(acc[1][4], Vf[7][1], B[1][4]);
  dma<3072,true,false>(regb + 12288, vV[3], rV, 180224u);
  mfmab<false>(acc[2][0], Vf[7][2], B[1][0]);
  mfmab<false>(acc[2][1], Vf[7][2], B[1][1]);
  mfmab<false>(acc[2][2], Vf[7][2], B[1][2]);
  mfmab<false>(acc[2][3], Vf[7][2], B[1][3]);
  mfmab<false>(acc[2][4], Vf[7][2], B[1][4]);
  mfmab<false>(acc[3][0], Vf[7][3], B[1][0]);
  mfmab<false>(acc[3][1], Vf[7][3], B[1][1]);
  dma<0,true,true>(regb + 16384, vV[0], rV, 245760u);
  mfmab<false>(acc[3][2], Vf[7][3], B[1][2]);
  mfmab<false>(acc[3][3], Vf[7][3], B[1][3]);
  mfmab<false>(acc[3][4], Vf[7][3], B[1][4]);
  wait_lgkm0();
  mfmab<false>(acc[0][0], Vf[8][0], B[0][0]);
  mfmab<false>(acc[0][1], Vf[8][0], B[0][1]);
  B[1][0] = ld128(lds, aB + 5120);
  B[1][1] = ld128(lds, aB + 6144);
  mfmab<false>(acc[0][2], Vf[8][0], B[0][2]);
  B[1][2] = ld128(lds, aB + 7168);
  dma<1024,true,false>(regb + 16384, vV[1], rV, 245760u);
  mfmab<false>(acc[0][3], Vf[8][0], B[0][3]);
  B[1][3] = ld128(lds, aB + 8192);
  B[1][4] = ld128(lds, aB + 9216);
  mfmab<false>(acc[0][4], Vf[8][0], B[0][4]);
  wait_vm<14>();
  Vf[11][0] = ldtr(lds, aV[0] + 4096, aV[0] + 6144);
  mfmab<false>(acc[1][0], Vf[8][1], B[0][0]);
  Vf[11][1] = ldtr(lds, aV[1] + 4096, aV[1] + 6144);
  mfmab<false>(acc[1][1], Vf[8][1], B[0][1]);
  mfmab<false>(acc[1][2], Vf[8][1], B[0][2]);
  Vf[11][2] = ldtr(lds, aV[2] + 4096, aV[2] + 6144);
  mfmab<false>(acc[1][3], Vf[8][1], B[0][3]);
  Vf[11][3] = ldtr(lds, aV[3] + 4096, aV[3] + 6144);
  mfmab<false>(acc[1][4], Vf[8][1], B[0][4]);
  dma<2048,true,false>(regb + 16384, vV[2], rV, 245760u);
  mfmab<false>(acc[2][0], Vf[8][2], B[0][0]);
  st128(lds, aP + 20480, P3[0]);
  mfmab<false>(acc[2][1], Vf[8][2], B[0][1]);
  mfmab<false>(acc[2][2], Vf[8][2], B[0][2]);
  st128(lds, aP + 21504, P3[1]);
  mfmab<false>(acc[2][3], Vf[8][2], B[0][3]);
  mfmab<false>(acc[2][4], Vf[8][2], B[0][4]);
  st128(lds, aP + 22528, P3[2]);
  mfmab<false>(acc[3][0], Vf[8][3], B[0][0]);
  dma<3072,true,false>(regb + 16384, vV[3], rV, 245760u);
  mfmab<false>(acc[3][1], Vf[8][3], B[0][1]);
  st128(lds, aP + 23552, P3[3]);
  mfmab<false>(acc[3][2], Vf[8][3], B[0][2]);
  mfmab<false>(acc[3][3], Vf[8][3], B[0][3]);
  st128(lds, aP + 24576, P3[4]);
  mfmab<false>(acc[3][4], Vf[8][3], B[0][4]);
  wait_lgkm0();
  mfmab<false>(acc[0][0], Vf[9][0], B[1][0]);
  mfmab<false>(acc[0][1], Vf[9][0], B[1][1]);
  B[0][0] = ld128(lds, aB + 10240);
  B[0][1] = ld128(lds, aB + 11264);
  mfmab<false>(acc[0][2], Vf[9][0], B[1][2]);
  B[0][2] = ld128(lds, aB + 12288);
  dma<0,false,true>(regb + 4096, vA[0], rO, OSOFF(0) + wave * 128);
  mfmab<false>(acc[0][3], Vf[9][0], B[1][3]);
  B[0][3] = ld128(lds, aB + 13312);
  B[0][4] = ld128(lds, aB + 14336);
  mfmab<false>(acc[0][4], Vf[9][0], B[1][4]);
  wait_vm<13>();
  Vf[12][0] = ldtr(lds, aV[0] + 0, aV[0] + 2048);
  mfmab<false>(acc[1][0], Vf[9][1], B[1][0]);
  Vf[12][1] = ldtr(lds, aV[1] + 0, aV[1] + 2048);
  mfmab<false>(acc[1][1], Vf[9][1], B[1][1]);
  mfmab<false>(acc[1][2], Vf[9][1], B[1][2]);
  Vf[12][2] = ldtr(lds, aV[2] + 0, aV[2] + 2048);
  mfmab<false>(acc[1][3], Vf[9][1], B[1][3]);
  Vf[12][3] = ldtr(lds, aV[3] + 0, aV[3] + 2048);
  dma<1024,false,false>(regb + 4096, vA[1], rO, OSOFF(0) + wave * 128);
  mfmab<false>(acc[1][4], Vf[9][1], B[1][4]);
  mfmab<false>(acc[2][0], Vf[9][2], B[1][0]);
  mfmab<false>(acc[2][1], Vf[9][2], B[1][1]);
  mfmab<false>(acc[2][2], Vf[9][2], B[1][2]);
  mfmab<false>(acc[2][3], Vf[9][2], B[1][3]);
  mfmab<false>(acc[2][4], Vf[9][2], B[1][4]);
  mfmab<false>(acc[3][0], Vf[9][3], B[1][0]);
  dma<2048,false,false>(regb + 4096, vA[2], rO, OSOFF(0) + wave * 128);
  mfmab<false>(acc[3][1], Vf[9][3], B[1][1]);
  mfmab<false>(acc[3][2], Vf[9][3], B[1][2]);
  mfmab<false>(acc[3][3], Vf[9][3], B[1][3]);
  mfmab<false>(acc[3][4], Vf[9][3], B[1][4]);
  wait_lgkm0();
  mfmab<false>(acc[0][0], Vf[10][0], B[0][0]);
  mfmab<false>(acc[0][1], Vf[10][0], B[0][1]);
  B[1][0] = ld128(lds, aB + 15360);
  B[1][1] = ld128(lds, aB + 16384);
  dma<3072,false,false>(regb + 4096, vA[3], rO, OSOFF(0) + wave * 128);
  mfmab<false>(acc[0][2], Vf[10][0], B[0][2]);
  B[1][2] = ld128(lds, aB + 17408);
  mfmab<false>(acc[0][3], Vf[10][0], B[0][3]);
  B[1][3] = ld128(lds, aB + 18432);
  B[1][4] = ld128(lds, aB + 19456);
  mfmab<false>(acc[0][4], Vf[10][0], B[0][4]);
  wait_vm<12>();
  Vf[13][0] = ldtr(lds, aV[0] + 8192, aV[0] + 10240);
  mfmab<false>(acc[1][0], Vf[10][1], B[0][0]);
  Vf[13][1] = ldtr(lds, aV[1] + 8192, aV[1] + 10240);
  mfmab<false>(acc[1][1], Vf[10][1], B[0][1]);
  mfmab<false>(acc[1][2], Vf[10][1], B[0][2]);
  Vf[13][2] = ldtr(lds, aV[2] + 8192, aV[2] + 10240);
  mfmab<false>(acc[1][3], Vf[10][1], B[0][3]);
  Vf[13][3] = ldtr(lds, aV[3] + 8192, aV[3] + 10240);
  dma<0,false,true>(regb + 0, vA[0], rO, OSOFF(1) + wave * 128);
  mfmab<false>(acc[1][4], Vf[10][1], B[0][4]);
  mfmab<false>(acc[2][0], Vf[10][2], B[0][0]);
  mfmab<false>(acc[2][1], Vf[10][2], B[0][1]);
  mfmab<false>(acc[2][2], Vf[10][2], B[0][2]);
  mfmab<false>(acc[2][3], Vf[10][2], B[0][3]);
  mfmab<false>(acc[2][4], Vf[10][2], B[0][4]);
  dma<1024,false,false>(regb + 0, vA[1], rO, OSOFF(1) + wave * 128);
  mfmab<false>(acc[3][0], Vf[10][3], B[0][0]);
  mfmab<false>(acc[3][1], Vf[10][3], B[0][1]);
  mfmab<false>(acc[3][2], Vf[10][3], B[0][2]);
  mfmab<false>(acc[3][3], Vf[10][3], B[0][3]);
  mfmab<false>(acc[3][4], Vf[10][3], B[0][4]);
  barrier();
  mfmab<false>(acc[0][0], Vf[11][0], B[1][0]);
  dma<2048,false,false>(regb + 0, vA[2], rO, OSOFF(1) + wave * 128);
  mfmab<false>(acc[0][1], Vf[11][0], B[1][1]);
  B[0][0] = ld128(lds, aB + 20480);
  B[0][1] = ld128(lds, aB + 21504);
  mfmab<false>(acc[0][2], Vf[11][0], B[1][2]);
  B[0][2] = ld128(lds, aB + 22528);
  mfmab<false>(acc[0][3], Vf[11][0], B[1][3]);
  B[0][3] = ld128(lds, aB + 23552);
  B[0][4] = ld128(lds, aB + 24576);
  mfmab<false>(acc[0][4], Vf[11][0], B[1][4]);
  wait_vm<11>();
  Vf[14][0] = ldtr(lds, aV[0] + 12288, aV[0] + 14336);
  mfmab<false>(acc[1][0], Vf[11][1], B[1][0]);
  Vf[14][1] = ldtr(lds, aV[1] + 12288, aV[1] + 14336);
  mfmab<false>(acc[1][1], Vf[11][1], B[1][1]);
  dma<3072,false,false>(regb + 0, vA[3], rO, OSOFF(1) + wave * 128);
  mfmab<false>(acc[1][2], Vf[11][1], B[1][2]);
  Vf[14][2] = ldtr(lds, aV[2] + 12288, aV[2] + 14336);
  mfmab<false>(acc[1][3], Vf[11][1], B[1][3]);
  Vf[14][3] = ldtr(lds, aV[3] + 12288, aV[3] + 14336);
  mfmab<false>(acc[1][4], Vf[11][1], B[1][4]);
  mfmab<false>(acc[2][0], Vf[11][2], B[1][0]);
  mfmab<false>(acc[2][1], Vf[11][2], B[1][1]);
  mfmab<false>(acc[2][2], Vf[11][2], B[1][2]);
  dma<0,false,true>(regb + 8192, vA[0], rO, OSOFF(2) + wave * 128);
  mfmab<false>(acc[2][3], Vf[11][2], B[1][3]);
  mfmab<false>(acc[2][4], Vf[11][2], B[1][4]);
  mfmab<false>(acc[3][0], Vf[11][3], B[1][0]);
  mfmab<false>(acc[3][1], Vf[11][3], B[1][1]);
  mfmab<false>(acc[3][2], Vf[11][3], B[1][2]);
  mfmab<false>(acc[3][3], Vf[11][3], B[1][3]);
  mfmab<false>(acc[3][4], Vf[11][3], B[1][4]);
  dma<1024,false,false>(regb + 8192, vA[1], rO, OSOFF(2) + wave * 128);
  wait_lgkm0();
  mfmab<false>(acc[0][0], Vf[12][0], B[0][0]);
  mfmab<false>(acc[0][1], Vf[12][0], B[0][1]);
  B[1][0] = ld128(lds, aB + 25600);
  mfmab<false>(acc[0][2], Vf[12][0], B[0][2]);
  B[1][1] = ld128(lds, aB + 26624);
  mfmab<false>(acc[0][3], Vf[12][0], B[0][3]);
  B[1][2] = ld128(lds, aB + 27648);
  mfmab<false>(acc[0][4], Vf[12][0], B[0][4]);
  B[1][3] = ld128(lds, aB + 28672);
  mfmab<false>(acc[1][0], Vf[12][1], B[0][0]);
  B[1][4] = ld128(lds, aB + 29696);
  dma<2048,false,false>(regb + 8192, vA[2], rO, OSOFF(2) + wave * 128);
  mfmab<false>(acc[1][1], Vf[12][1], B[0][1]);
  mfmab<false>(acc[1][2], Vf[12][1], B[0][2]);
  mfmab<false>(acc[1][3], Vf[12][1], B[0][3]);
  dma<3072,false,false>(regb + 8192, vA[3], rO, OSOFF(2) + wave * 128);
  mfmab<false>(acc[1][4], Vf[12][1], B[0][4]);
  mfmab<false>(acc[2][0], Vf[12][2], B[0][0]);
  mfmab<false>(acc[2][1], Vf[12][2], B[0][1]);
  dma<0,false,true>(regb + 12288, vA[0], rO, OSOFF(3) + wave * 128);
  mfmab<false>(acc[2][2], Vf[12][2], B[0][2]);
  mfmab<false>(acc[2][3], Vf[12][2], B[0][3]);
  mfmab<false>(acc[2][4], Vf[12][2], B[0][4]);
  mm[0] = *(LAS f32x4*)(lds + aSr + ST_SUM + 0);
  dma<1024,false,false>(regb + 12288, vA[1], rO, OSOFF(3) + wave * 128);
  mfmab<false>(acc[3][0], Vf[12][3], B[0][0]);
  mm[1] = *(LAS f32x4*)(lds + aSr + ST_SUM + 256);
  mfmab<false>(acc[3][1], Vf[12][3], B[0][1]);
  mm[2] = *(LAS f32x4*)(lds + aSr + ST_SUM + 512);
  mfmab<false>(acc[3][2], Vf[12][3], B[0][2]);
  dma<2048,false,false>(regb + 12288, vA[2], rO, OSOFF(3) + wave * 128);
  mfmab<false>(acc[3][3], Vf[12][3], B[0][3]);
  mm[3] = *(LAS f32x4*)(lds + aSr + ST_SUM + 768);
  mfmab<false>(acc[3][4], Vf[12][3], B[0][4]);
  mm[4] = *(LAS f32x4*)(lds + aSr + ST_SUM + 1024);
  wait_lgkm0();
  mfmab<false>(acc[0][0], Vf[13][0], B[1][0]);
  t0_[0] = vaddr(mm[0][0], mm[0][1]);
  t0_[1] = vaddr(mm[1][0], mm[1][1]);
  dma<3072,false,false>(regb + 12288, vA[3], rO, OSOFF(3) + wave * 128);
  mfmab<false>(acc[0][1], Vf[13][0], B[1][1]);
  B[0][0] = ld128(lds, aB + 30720);
  B[0][1] = ld128(lds, aB + 31744);
  t0_[2] = vaddr(mm[2][0], mm[2][1]);
  mfmab<false>(acc[0][2], Vf[13][0], B[1][2]);
  B[0][2] = ld128(lds, aB + 32768);
  B[0][3] = ld128(lds, aB + 33792);
  mfmab<false>(acc[0][3], Vf[13][0], B[1][3]);
  B[0][4] = ld128(lds, aB + 34816);
  t0_[3] = vaddr(mm[3][0], mm[3][1]);
  mfmab<false>(acc[0][4], Vf[13][0], B[1][4]);
  wait_vm<16>();
  Vf[15][0] = ldtr(lds, aV[0] + 16384, aV[0] + 18432);
  dma<0,false,true>(regb + 20480, vA[0], rO, OSOFF(4) + wave * 128);
  mfmab<false>(acc[1][0], Vf[13][1], B[1][0]);
  Vf[15][1] = ldtr(lds, aV[1] + 16384, aV[1] + 18432);
  t0_[4] = vaddr(mm[4][0], mm[4][1]);
  mfmab<false>(acc[1][1], Vf[13][1], B[1][1]);
  Vf[15][2] = ldtr(lds, aV[2] + 16384, aV[2] + 18432);
  mfmab<false>(acc[1][2], Vf[13][1], B[1][2]);
  Vf[15][3] = ldtr(lds, aV[3] + 16384, aV[3] + 18432);
  mfmab<false>(acc[1][3], Vf[13][1], B[1][3]);
  t1_[0] = vaddr(mm[0][2], mm[0][3]);
  t1_[1] = vaddr(mm[1][2], mm[1][3]);
  t1_[2] = vaddr(mm[2][2], mm[2][3]);
  mfmab<false>(acc[1][4], Vf[13][1], B[1][4]);
  wait_vm<13>();
  Wf[0][0] = ldtr(lds, aV[0] + 4096, aV[0] + 6144);
  mfmab<false>(acc[2][0], Vf[13][2], B[1][0]);
  Wf[0][1] = ldtr(lds, aV[1] + 4096, aV[1] + 6144);
  mfmab<false>(acc[2][1], Vf[13][2], B[1][1]);
  Wf[0][2] = ldtr(lds, aV[2] + 4096, aV[2] + 6144);
  t1_[3] = vaddr(mm[3][2], mm[3][3]);
  dma<1024,false,false>(regb + 20480, vA[1], rO, OSOFF(4) + wave * 128);
  mfmab<false>(acc[2][2], Vf[13][2], B[1][2]);
  Wf[0][3] = ldtr(lds, aV[3] + 4096, aV[3] + 6144);
  mfmab<false>(acc[2][3], Vf[13][2], B[1][3]);
  t1_[4] = vaddr(mm[4][2], mm[4][3]);
  t0_[0] = vaddr(t0_[0], t1_[0]);
  mfmab<false>(acc[2][4], Vf[13][2], B[1][4]);
  t0_[1] = vaddr(t0_[1], t1_[1]);
  t0_[2] = vaddr(t0_[2], t1_[2]);
  mfmab<false>(acc[3][0], Vf[13][3], B[1][0]);
  t0_[3] = vaddr(t0_[3], t1_[3]);
  t0_[4] = vaddr(t0_[4], t1_[4]);
  mfmab<false>(acc[3][1], Vf[13][3], B[1][1]);
  rl[0] = vrcp(t0_[0]);
  rl[1] = vrcp(t0_[1]);
  mfmab<false>(acc[3][2], Vf[13][3], B[1][2]);
  rl[2] = vrcp(t0_[2]);
  dma<2048,false,false>(regb + 20480, vA[2], rO, OSOFF(4) + wave * 128);
  mfmab<false>(acc[3][3], Vf[13][3], B[1][3]);
  rl[3] = vrcp(t0_[3]);
  mfmab<false>(acc[3][4], Vf[13][3], B[1][4]);
  rl[4] = vrcp(t0_[4]);
  wait_lgkm0();
  mfmab<false>(acc[0][0], Vf[14][0], B[0][0]);
  mfmab<false>(acc[0][1], Vf[14][0], B[0][1]);
  B[1][0] = ld128(lds, aB + 35840);
  B[1][1] = ld128(lds, aB + 36864);
  mfmab<false>(acc[0][2], Vf[14][0], B[0][2]);
  B[1][2] = ld128(lds, aB + 37888);
  dma<3072,false,false>(regb + 20480, vA[3], rO, OSOFF(4) + wave * 128);
  mfmab<false>(acc[0][3], Vf[14][0], B[0][3]);
  B[1][3] = ld128(lds, aB + 38912);
  B[1][4] = ld128(lds, aB + 39936);
  mfmab<false>(acc[0][4], Vf[14][0], B[0][4]);
  wait_vm<12>();
  Wf[1][0] = ldtr(lds, aV[0] + 0, aV[0] + 2048);
  mfmab<false>(acc[1][0], Vf[14][1], B[0][0]);
  Wf[1][1] = ldtr(lds, aV[1] + 0, aV[1] + 2048);
  mfmab<false>(acc[1][1], Vf[14][1], B[0][1]);
  mfmab<false>(acc[1][2], Vf[14][1], B[0][2]);
  Wf[1][2] = ldtr(lds, aV[2] + 0, aV[2] + 2048);
  mfmab<false>(acc[1][3], Vf[14][1], B[0][3]);
  Wf[1][3] = ldtr(lds, aV[3] + 0, aV[3] + 2048);
  mfmab<false>(acc[1][4], Vf[14][1], B[0][4]);
  dma<0,false,true>(regb + 4096, vA[0], rO, OSOFF(5) + wave * 128);
  mfmab<false>(acc[2][0], Vf[14][2], B[0][0]);
  mfmab<false>(acc[2][1], Vf[14][2], B[0][1]);
  mfmab<false>(acc[2][2], Vf[14][2], B[0][2]);
  mfmab<false>(acc[2][3], Vf[14][2], B[0][3]);
  mfmab<false>(acc[2][4], Vf[14][2], B[0][4]);
  mfmab<false>(acc[3][0], Vf[14][3], B[0][0]);
  dma<1024,false,false>(regb + 4096, vA[1], rO, OSOFF(5) + wave * 128);
  mfmab<false>(acc[3][1], Vf[14][3], B[0][1]);
  mfmab<false>(acc[3][2], Vf[14][3], B[0][2]);
  mfmab<false>(acc[3][3], Vf[14][3], B[0][3]);
  dma<2048,false,false>(regb + 4096, vA[2], rO, OSOFF(5) + wave * 128);
  mfmab<false>(acc[3][4], Vf[14][3], B[0][4]);
  barrier();
  mfmab<false>(acc[0][0], Vf[15][0], B[1][0]);
  dma<3072,false,false>(regb + 4096, vA[3], rO, OSOFF(5) + wave * 128);
  mfmab<false>(acc[1][0], Vf[15][1], B[1][0]);
  wait_vm<12>();
  Wf[2][0] = ldtr(lds, aV[0] + 8192, aV[0] + 10240);
  mfmab<false>(acc[2][0], Vf[15][2], B[1][0]);
  Wf[2][1] = ldtr(lds, aV[1] + 8192, aV[1] + 10240);
  mfmab<false>(acc[3][0], Vf[15][3], B[1][0]);
  Wf[2][2] = ldtr(lds, aV[2] + 8192, aV[2] + 10240);
  dma<0,false,true>(regb + 0, vA[0], rO, OSOFF(6) + wave * 128);
  mfmab<false>(acc[0][1], Vf[15][0], B[1][1]);
  Wf[2][3] = ldtr(lds, aV[3] + 8192, aV[3] + 10240);
  mfmab<false>(acc[1][1], Vf[15][1], B[1][1]);
  wait_vm<9>();
  Wf[3][0] = ldtr(lds, aV[0] + 12288, aV[0] + 14336);
  dma<1024,false,false>(regb + 0, vA[1], rO, OSOFF(6) + wave * 128);
  mfmab<false>(acc[2][1], Vf[15][2], B[1][1]);
  Wf[3][1] = ldtr(lds, aV[1] + 12288, aV[1] + 14336);
  dma<2048,false,false>(regb + 0, vA[2], rO, OSOFF(6) + wave * 128);
  mfmab<false>(acc[3][1], Vf[15][3], B[1][1]);
  Wf[3][2] = ldtr(lds, aV[2] + 12288, aV[2] + 14336);
  pq[0][0] = vcvt(acc[0][0][0], acc[0][0][1]);
  pq[0][1] = vcvt(acc[0][0][2], acc[0][0][3]);
  pq[0][2] = vcvt(acc[1][0][0], acc[1][0][1]);
  mfmab<false>(acc[0][2], Vf[15][0], B[1][2]);
  Wf[3][3] = ldtr(lds, aV[3] + 12288, aV[3] + 14336);
  pq[0][3] = vcvt(acc[1][0][2], acc[1][0][3]);
  st128(lds, aOw[0] + 0, pq[0]);
  dma<3072,false,false>(regb + 0, vA[3], rO, OSOFF(6) + wave * 128);
  mfmab<false>(acc[1][2], Vf[15][1], B[1][2]);
  pq[1][0] = vcvt(acc[2][0][0], acc[2][0][1]);
  pq[1][1] = vcvt(acc[2][0][2], acc[2][0][3]);
  pq[1][2] = vcvt(acc[3][0][0], acc[3][0][1]);
  dma<0,false,true>(regb + 16384, vA[0], rO, OSOFF(7) + wave * 128);
  mfmab<false>(acc[2][2], Vf[15][2], B[1][2]);
  pq[1][3] = vcvt(acc[3][0][2], acc[3][0][3]);
  st128(lds, aOw[1] + 0, pq[1]);
  mfmab<false>(acc[3][2], Vf[15][3], B[1][2]);
  pq[0][0] = vcvt(acc[0][1][0], acc[0][1][1]);
  pq[0][1] = vcvt(acc[0][1][2], acc[0][1][3]);
  pq[0][2] = vcvt(acc[1][1][0], acc[1][1][1]);
  mfmab<false>(acc[0][3], Vf[15][0], B[1][3]);
  pq[0][3] = vcvt(acc[1][1][2], acc[1][1][3]);
  st128(lds, aOw[0] + 1024, pq[0]);
  dma<1024,false,false>(regb + 16384, vA[1], rO, OSOFF(7) + wave * 128);
  mfmab<false>(acc[1][3], Vf[15][1], B[1][3]);
  pq[1][0] = vcvt(acc[2][1][0], acc[2][1][1]);
  pq[1][1] = vcvt(acc[2][1][2], acc[2][1][3]);
  pq[1][2] = vcvt(acc[3][1][0], acc[3][1][1]);
  mfmab<false>(acc[2][3], Vf[15][2], B[1][3]);
  pq[1][3] = vcvt(acc[3][1][2], acc[3][1][3]);
  st128(lds, aOw[1] + 1024, pq[1]);
  dma<2048,false,false>(regb + 16384, vA[2], rO, OSOFF(7) + wave * 128);
  mfmab<false>(acc[3][3], Vf[15][3], B[1][3]);
  pq[0][0] = vcvt(acc[0][2][0], acc[0][2][1]);
  pq[0][1] = vcvt(acc[0][2][2], acc[0][2][3]);
  pq[0][2] = vcvt(acc[1][2][0], acc[1][2][1]);
  mfmab<false>(acc[0][4], Vf[15][0], B[1][4]);
  pq[0][3] = vcvt(acc[1][2][2], acc[1][2][3]);
  st128(lds, aOw[0] + 2048, pq[0]);
  dma<3072,false,false>(regb + 16384, vA[3], rO, OSOFF(7) + wave * 128);
  mfmab<false>(acc[1][4], Vf[15][1], B[1][4]);
  pq[1][0] = vcvt(acc[2][2][0], acc[2][2][1]);
  pq[1][1] = vcvt(acc[2][2][2], acc[2][2][3]);
  pq[1][2] = vcvt(acc[3][2][0], acc[3][2][1]);
  mfmab<false>(acc[2][4], Vf[15][2], B[1][4]);
  pq[1][3] = vcvt(acc[3][2][2], acc[3][2][3]);
  st128(lds, aOw[1] + 2048, pq[1]);
  dma<0,false,true>(regb + 24576, vA[0], rO, OSOFF(wave * 2 + 0) + 512);
  mfmab<false>(acc[3][4], Vf[15][3], B[1][4]);
  pq[0][0] = vcvt(acc[0][3][0], acc[0][3][1]);
  pq[0][1] = vcvt(acc[0][3][2], acc[0][3][3]);
  pq[0][2] = vcvt(acc[1][3][0], acc[1][3][1]);
  mfma_pad();
  pq[0][3] = vcvt(acc[1][3][2], acc[1][3][3]);
  st128(lds, aOw[0] + 3072, pq[0]);
  pq[1][0] = vcvt(acc[2][3][0], acc[2][3][1]);
  pq[1][1] = vcvt(acc[2][3][2], acc[2][3][3]);
  pq[1][2] = vcvt(acc[3][3][0], acc[3][3][1]);
  pq[1][3] = vcvt(acc[3][3][2], acc[3][3][3]);
  st128(lds, aOw[1] + 3072, pq[1]);
  pq[0][0] = vcvt(acc[0][4][0], acc[0][4][1]);
  dma<1024,false,false>(regb + 24576, vA[1], rO, OSOFF(wave * 2 + 0) + 512);
  pq[0][1] = vcvt(acc[0][4][2], acc[0][4][3]);
  pq[0][2] = vcvt(acc[1][4][0], acc[1][4][1]);
  pq[0][3] = vcvt(acc[1][4][2], acc[1][4][3]);
  st128(lds, aOw[0] + 4096, pq[0]);
  pq[1][0] = vcvt(acc[2][4][0], acc[2][4][1]);
  pq[1][1] = vcvt(acc[2][4][2], acc[2][4][3]);
  pq[1][2] = vcvt(acc[3][4][0], acc[3][4][1]);
  pq[1][3] = vcvt(acc[3][4][2], acc[3][4][3]);
  dma<2048,false,false>(regb + 24576, vA[2], rO, OSOFF(wave * 2 + 0) + 512);
  st128(lds, aOw[1] + 4096, pq[1]);
  barrier();
  F[0][0] = ld128(lds, aB + 0);
  F[0][1] = ld128(lds, aB + 1024);
  F[0][2] = ld128(lds, aB + 2048);
  F[0][3] = ld128(lds, aB + 3072);
  F[0][4] = ld128(lds, aB + 4096);
  SETUP_B(24576, 8192, 12288, 0, 4096, 16384);
  dma<3072,false,false>(regb + 24576, vA[3], rO, OSOFF(wave * 2 + 0) + 512);
  mfmav<true>(acc[0][0], Wf[0][0], F[0][0]);
  F[1][0] = ld128(lds, aB + 5120);
  F[1][1] = ld128(lds, aB + 6144);
  dma<0,false,true>(regb + 8192, vA[0], rO, OSOFF(wave * 2 + 0) + 640);
  mfmav<true>(acc[0][1], Wf[0][0], F[0][1]);
  F[1][2] = ld128(lds, aB + 7168);
  F[1][3] = ld128(lds, aB + 8192);
  dma<1024,false,false>(regb + 8192, vA[1], rO, OSOFF(wave * 2 + 0) + 640);
  mfmav<true>(acc[0][2], Wf[0][0], F[0][2]);
  F[1][4] = ld128(lds, aB + 9216);
  F[2][0] = ld128(lds, aB + 10240);
  dma<2048,false,false>(regb + 8192, vA[2], rO, OSOFF(wave * 2 + 0) + 640);
  mfmav<true>(acc[0][3], Wf[0][0], F[0][3]);
  F[2][1] = ld128(lds, aB + 11264);
  F[2][2] = ld128(lds, aB + 12288);
  dma<3072,false,false>(regb + 8192, vA[3], rO, OSOFF(wave * 2 + 0) + 640);
  mfmav<true>(acc[0][4], Wf[0][0], F[0][4]);
  F[2][3] = ld128(lds, aB + 13312);
  F[2][4] = ld128(lds, aB + 14336);
  mfmav<false>(acc[0][0], Wf[1][0], F[1][0]);
  F[3][0] = ld128(lds, aB + 15360);
  F[3][1] = ld128(lds, aB + 16384);
  mfmav<false>(acc[0][1], Wf[1][0], F[1][1]);
  F[3][2] = ld128(lds, aB + 17408);
  F[3][3] = ld128(lds, aB + 18432);
  dma<0,false,true>(regb + 12288, vA[0], rO, OSOFF(wave * 2 + 0) + 768);
  mfmav<false>(acc[0][2], Wf[1][0], F[1][2]);
  F[3][4] = ld128(lds, aB + 19456);
  F[4][0] = ld128(lds, aB + 20480);
  mfmav<false>(acc[0][3], Wf[1][0], F[1][3]);
  F[4][1] = ld128(lds, aB + 21504);
  F[4][2] = ld128(lds, aB + 22528);
  dma<1024,false,false>(regb + 12288, vA[1], rO, OSOFF(wave * 2 + 0) + 768);
  mfmav<false>(acc[0][4], Wf[1][0], F[1][4]);
  F[4][3] = ld128(lds, aB + 23552);
  F[4][4] = ld128(lds, aB + 24576);
  mfmav<false>(acc[0][0], Wf[2][0], F[2][0]);
  F[5][0] = ld128(lds, aB + 25600);
  F[5][1] = ld128(lds, aB + 26624);
  mfmav<false>(acc[0][1], Wf[2][0], F[2][1]);
  F[5][2] = ld128(lds, aB + 27648);
  F[5][3] = ld128(lds, aB + 28672);
  dma<2048,false,false>(regb + 12288, vA[2], rO, OSOFF(wave * 2 + 0) + 768);
  mfmav<false>(acc[0][2], Wf[2][0], F[2][2]);
  F[5][4] = ld128(lds, aB + 29696);
  mfmav<false>(acc[0][3], Wf[2][0], F[2][3]);
  wait_vm<23>();
  Wf[4][0] = ldtr(lds, aV[0] + 20480, aV[0] + 22528);
  Wf[4][1] = ldtr(lds, aV[1] + 20480, aV[1] + 22528);
  Wf[4][2] = ldtr(lds, aV[2] + 20480, aV[2] + 22528);
  Wf[4][3] = ldtr(lds, aV[3] + 20480, aV[3] + 22528);
  wait_vm<19>();
  Wf[5][0] = ldtr(lds, aV[0] + 4096, aV[0] + 6144);
  Wf[5][1] = ldtr(lds, aV[1] + 4096, aV[1] + 6144);
  mfmav<false>(acc[0][4], Wf[2][0], F[2][4]);
  Wf[5][2] = ldtr(lds, aV[2] + 4096, aV[2] + 6144);
  Wf[5][3] = ldtr(lds, aV[3] + 4096, aV[3] + 6144);
  wait_vm<15>();
  Wf[6][0] = ldtr(lds, aV[0] + 0, aV[0] + 2048);
  Wf[6][1] = ldtr(lds, aV[1] + 0, aV[1] + 2048);
  Wf[6][2] = ldtr(lds, aV[2] + 0, aV[2] + 2048);
  dma<3072,false,false>(regb + 12288, vA[3], rO, OSOFF(wave * 2 + 0) + 768);
  mfmav<false>(acc[0][0], Wf[3][0], F[3][0]);
  Wf[6][3] = ldtr(lds, aV[3] + 0, aV[3] + 2048);
  wait_vm<12>();
  Wf[7][0] = ldtr(lds, aV[0] + 16384, aV[0] + 18432);
  Wf[7][1] = ldtr(lds, aV[1] + 16384, aV[1] + 18432);
  Wf[7][2] = ldtr(lds, aV[2] + 16384, aV[2] + 18432);
  Wf[7][3] = ldtr(lds, aV[3] + 16384, aV[3] + 18432);
  mfmav<false>(acc[0][1], Wf[3][0], F[3][1]);
  mfmav<false>(acc[0][2], Wf[3][0], F[3][2]);
  mfmav<false>(acc[0][3], Wf[3][0], F[3][3]);
  mfmav<false>(acc[0][4], Wf[3][0], F[3][4]);
  wait_lgkm0();
  mfmav<false>(acc[0][0], Wf[4][0], F[4][0]);
  F[6][0] = ld128(lds, aB + 30720);
  F[6][1] = ld128(lds, aB + 31744);
  dma<0,false,true>(regb + 0, vA[0], rO, OSOFF(wave * 2 + 1) + 512);
  mfmav<false>(acc[0][1], Wf[4][0], F[4][1]);
  F[6][2] = ld128(lds, aB + 32768);
  F[6][3] = ld128(lds, aB + 33792);
  dma<1024,false,false>(regb + 0, vA[1], rO, OSOFF(wave * 2 + 1) + 512);
  mfmav<false>(acc[0][2], Wf[4][0], F[4][2]);
  F[6][4] = ld128(lds, aB + 34816);
  F[7][0] = ld128(lds, aB + 35840);
  dma<2048,false,false>(regb + 0, vA[2], rO, OSOFF(wave * 2 + 1) + 512);
  mfmav<false>(acc[0][3], Wf[4][0], F[4][3]);
  F[7][1] = ld128(lds, aB + 36864);
  F[7][2] = ld128(lds, aB + 37888);
  dma<3072,false,false>(regb + 0, vA[3], rO, OSOFF(wave * 2 + 1) + 512);
  mfmav<false>(acc[0][4], Wf[4][0], F[4][4]);
  F[7][3] = ld128(lds, aB + 38912);
  F[7][4] = ld128(lds, aB + 39936);
  dma<0,false,true>(regb + 4096, vA[0], rO, OSOFF(wave * 2 + 1) + 640);
  mfmav<false>(acc[0][0], Wf[5][0], F[5][0]);
  dma<1024,false,false>(regb + 4096, vA[1], rO, OSOFF(wave * 2 + 1) + 640);
  mfmav<false>(acc[0][1], Wf[5][0], F[5][1]);
  mfmav<false>(acc[0][2], Wf[5][0], F[5][2]);
  dma<2048,false,false>(regb + 4096, vA[2], rO, OSOFF(wave * 2 + 1) + 640);
  mfmav<false>(acc[0][3], Wf[5][0], F[5][3]);
  mfmav<false>(acc[0][4], Wf[5][0], F[5][4]);
  mfmav<false>(acc[0][0], Wf[6][0], F[6][0]);
  dma<3072,false,false>(regb + 4096, vA[3], rO, OSOFF(wave * 2 + 1) + 640);
  mfmav<false>(acc[0][1], Wf[6][0], F[6][1]);
  mfmav<false>(acc[0][2], Wf[6][0], F[6][2]);
  mfmav<false>(acc[0][3], Wf[6][0], F[6][3]);
  dma<0,false,true>(regb + 16384, vA[0], rO, OSOFF(wave * 2 + 1) + 768);
  mfmav<false>(acc[0][4], Wf[6][0], F[6][4]);
  mfmav<false>(acc[0][0], Wf[7][0], F[7][0]);
  mfmav<false>(acc[0][1], Wf[7][0], F[7][1]);
  dma<1024,false,false>(regb + 16384, vA[1], rO, OSOFF(wave * 2 + 1) + 768);
  mfmav<false>(acc[0][2], Wf[7][0], F[7][2]);
  mfmav<false>(acc[0][3], Wf[7][0], F[7][3]);
  mfmav<false>(acc[0][4], Wf[7][0], F[7][4]);
  dma<2048,false,false>(regb + 16384, vA[2], rO, OSOFF(wave * 2 + 1) + 768);
  wait_lgkm0();
  mfmav<true>(acc[1][0], Wf[0][1], F[0][0]);
  mfmav<true>(acc[1][1], Wf[0][1], F[0][1]);
  mfmav<true>(acc[1][2], Wf[0][1], F[0][2]);
  z[0] = vmul(acc[0][0][0], rl[0]);
  mfmav<true>(acc[1][3], Wf[0][1], F[0][3]);
  dma<3072,false,false>(regb + 16384, vA[3], rO, OSOFF(wave * 2 + 1) + 768);
  mfmav<true>(acc[1][4], Wf[0][1], F[0][4]);
  z[1] = vmul(acc[0][0][1], rl[0]);
  mfmav<false>(acc[1][0], Wf[1][1], F[1][0]);
  z[2] = vmul(acc[0][0][2], rl[0]);
  mfmav<false>(acc[1][1], Wf[1][1], F[1][1]);
  z[3] = vmul(acc[0][0][3], rl[0]);
  mfmav<false>(acc[1][2], Wf[1][1], F[1][2]);
  mfmav<false>(acc[1][3], Wf[1][1], F[1][3]);
  z[4] = vmul(acc[0][1][0], rl[1]);
  mfmav<false>(acc[1][4], Wf[1][1], F[1][4]);
  z[5] = vmul(acc[0][1][1], rl[1]);
  mfmav<false>(acc[1][0], Wf[2][1], F[2][0]);
  mfmav<false>(acc[1][1], Wf[2][1], F[2][1]);
  z[6] = vmul(acc[0][1][2], rl[1]);
  mfmav<false>(acc[1][2], Wf[2][1], F[2][2]);
  z[7] = vmul(acc[0][1][3], rl[1]);
  mfmav<false>(acc[1][3], Wf[2][1], F[2][3]);
  pk[0] = vcvt(z[0], z[1]);
  mfmav<false>(acc[1][4], Wf[2][1], F[2][4]);
  mfmav<false>(acc[1][0], Wf[3][1], F[3][0]);
  pk[1] = vcvt(z[2], z[3]);
  mfmav<false>(acc[1][1], Wf[3][1], F[3][1]);
  pk[2] = vcvt(z[4], z[5]);
  mfmav<false>(acc[1][2], Wf[3][1], F[3][2]);
  pk[3] = vcvt(z[6], z[7]);
  mfmav<false>(acc[1][3], Wf[3][1], F[3][3]);
  mfmav<false>(acc[1][4], Wf[3][1], F[3][4]);
  __builtin_amdgcn_raw_buffer_store_b128(u32x4{pk[0], pk[1], pk[2], pk[3]}, rP, pb + 0, 0, 16);
  wait_lgkm0();
  mfmav<false>(acc[1][0], Wf[4][1], F[4][0]);
  z[0] = vmul(acc[0][2][0], rl[2]);
  mfmav<false>(acc[1][1], Wf[4][1], F[4][1]);
  mfmav<false>(acc[1][2], Wf[4][1], F[4][2]);
  z[1] = vmul(acc[0][2][1], rl[2]);
  mfmav<false>(acc[1][3], Wf[4][1], F[4][3]);
  z[2] = vmul(acc[0][2][2], rl[2]);
  mfmav<false>(acc[1][4], Wf[4][1], F[4][4]);
  mfmav<false>(acc[1][0], Wf[5][1], F[5][0]);
  z[3] = vmul(acc[0][2][3], rl[2]);
  mfmav<false>(acc[1][1], Wf[5][1], F[5][1]);
  z[4] = vmul(acc[0][3][0], rl[3]);
  mfmav<false>(acc[1][2], Wf[5][1], F[5][2]);
  mfmav<false>(acc[1][3], Wf[5][1], F[5][3]);
  z[5] = vmul(acc[0][3][1], rl[3]);
  mfmav<false>(acc[1][4], Wf[5][1], F[5][4]);
  z[6] = vmul(acc[0][3][2], rl[3]);
  mfmav<false>(acc[1][0], Wf[6][1], F[6][0]);
  mfmav<false>(acc[1][1], Wf[6][1], F[6][1]);
  z[7] = vmul(acc[0][3][3], rl[3]);
  mfmav<false>(acc[1][2], Wf[6][1], F[6][2]);
  mfmav<false>(acc[1][3], Wf[6][1], F[6][3]);
  pk[0] = vcvt(z[0], z[1]);
  mfmav<false>(acc[1][4], Wf[6][1], F[6][4]);
  pk[1] = vcvt(z[2], z[3]);
  mfmav<false>(acc[1][0], Wf[7][1], F[7][0]);
  mfmav<false>(acc[1][1], Wf[7][1], F[7][1]);
  pk[2] = vcvt(z[4], z[5]);
  mfmav<false>(acc[1][2], Wf[7][1], F[7][2]);
  pk[3] = vcvt(z[6], z[7]);
  mfmav<false>(acc[1][3], Wf[7][1], F[7][3]);
  mfmav<false>(acc[1][4], Wf[7][1], F[7][4]);
  __builtin_amdgcn_raw_buffer_store_b128(u32x4{pk[0], pk[1], pk[2], pk[3]}, rP, pb + 4096, 0, 16);
  wait_lgkm0();
  mfmav<true>(acc[2][0], Wf[0][2], F[0][0]);
  mfmav<true>(acc[2][1], Wf[0][2], F[0][1]);
  mfmav<true>(acc[2][2], Wf[0][2], F[0][2]);
  z[0] = vmul(acc[0][4][0], rl[4]);
  mfmav<true>(acc[2][3], Wf[0][2], F[0][3]);
  z[1] = vmul(acc[0][4][1], rl[4]);
  mfmav<true>(acc[2][4], Wf[0][2], F[0][4]);
  z[2] = vmul(acc[0][4][2], rl[4]);
  mfmav<false>(acc[2][0], Wf[1][2], F[1][0]);
  z[3] = vmul(acc[0][4][3], rl[4]);
  mfmav<false>(acc[2][1], Wf[1][2], F[1][1]);
  z[4] = vmul(acc[1][0][0], rl[0]);
  z[5] = vmul(acc[1][0][1], rl[0]);
  mfmav<false>(acc[2][2], Wf[1][2], F[1][2]);
  z[6] = vmul(acc[1][0][2], rl[0]);
  mfmav<false>(acc[2][3], Wf[1][2], F[1][3]);
  z[7] = vmul(acc[1][0][3], rl[0]);
  mfmav<false>(acc[2][4], Wf[1][2], F[1][4]);
  pk[0] = vcvt(z[0], z[1]);
  mfmav<false>(acc[2][0], Wf[2][2], F[2][0]);
  pk[1] = vcvt(z[2], z[3]);
  mfmav<false>(acc[2][1], Wf[2][2], F[2][1]);
  pk[2] = vcvt(z[4], z[5]);
  mfmav<false>(acc[2][2], Wf[2][2], F[2][2]);
  pk[3] = vcvt(z[6], z[7]);
  mfmav<false>(acc[2][3], Wf[2][2], F[2][3]);
  __builtin_amdgcn_raw_buffer_store_b128(u32x4{pk[0], pk[1], pk[2], pk[3]}, rP, pb + 8192, 0, 16);
  mfmav<false>(acc[2][4], Wf[2][2], F[2][4]);
  z[0] = vmul(acc[1][1][0], rl[1]);
  mfmav<false>(acc[2][0], Wf[3][2], F[3][0]);
  z[1] = vmul(acc[1][1][1], rl[1]);
  z[2] = vmul(acc[1][1][2], rl[1]);
  mfmav<false>(acc[2][1], Wf[3][2], F[3][1]);
  z[3] = vmul(acc[1][1][3], rl[1]);
  mfmav<false>(acc[2][2], Wf[3][2], F[3][2]);
  z[4] = vmul(acc[1][2][0], rl[2]);
  mfmav<false>(acc[2][3], Wf[3][2], F[3][3]);
  z[5] = vmul(acc[1][2][1], rl[2]);
  mfmav<false>(acc[2][4], Wf[3][2], F[3][4]);
  z[6] = vmul(acc[1][2][2], rl[2]);
  wait_lgkm0();
  mfmav<false>(acc[2][0], Wf[4][2], F[4][0]);
  z[7] = vmul(acc[1][2][3], rl[2]);
  mfmav<false>(acc[2][1], Wf[4][2], F[4][1]);
  pk[0] = vcvt(z[0], z[1]);
  mfmav<false>(acc[2][2], Wf[4][2], F[4][2]);
  pk[1] = vcvt(z[2], z[3]);
  mfmav<false>(acc[2][3], Wf[4][2], F[4][3]);
  pk[2] = vcvt(z[4], z[5]);
  mfmav<false>(acc[2][4], Wf[4][2], F[4][4]);
  pk[3] = vcvt(z[6], z[7]);
  mfmav<false>(acc[2][0], Wf[5][2], F[5][0]);
  __builtin_amdgcn_raw_buffer_store_b128(u32x4{pk[0], pk[1], pk[2], pk[3]}, rP, pb + 12288, 0, 16);
  mfmav<false>(acc[2][1], Wf[5][2], F[5][1]);
  z[0] = vmul(acc[1][3][0], rl[3]);
  mfmav<false>(acc[2][2], Wf[5][2], F[5][2]);
  z[1] = vmul(acc[1][3][1], rl[3]);
  mfmav<false>(acc[2][3], Wf[5][2], F[5][3]);
  z[2] = vmul(acc[1][3][2], rl[3]);
  mfmav<false>(acc[2][4], Wf[5][2], F[5][4]);
  mfmav<false>(acc[2][0], Wf[6][2], F[6][0]);
  z[3] = vmul(acc[1][3][3], rl[3]);
  mfmav<false>(acc[2][1], Wf[6][2], F[6][1]);
  z[4] = vmul(acc[1][4][0], rl[4]);
  mfmav<false>(acc[2][2], Wf[6][2], F[6][2]);
  z[5] = vmul(acc[1][4][1], rl[4]);
  mfmav<false>(acc[2][3], Wf[6][2], F[6][3]);
  z[6] = vmul(acc[1][4][2], rl[4]);
  mfmav<false>(acc[2][4], Wf[6][2], F[6][4]);
  z[7] = vmul(acc[1][4][3], rl[4]);
  mfmav<false>(acc[2][0], Wf[7][2], F[7][0]);
  pk[0] = vcvt(z[0], z[1]);
  mfmav<false>(acc[2][1], Wf[7][2], F[7][1]);
  pk[1] = vcvt(z[2], z[3]);
  mfmav<false>(acc[2][2], Wf[7][2], F[7][2]);
  pk[2] = vcvt(z[4], z[5]);
  mfmav<false>(acc[2][3], Wf[7][2], F[7][3]);
  pk[3] = vcvt(z[6], z[7]);
  mfmav<false>(acc[2][4], Wf[7][2], F[7][4]);
  __builtin_amdgcn_raw_buffer_store_b128(u32x4{pk[0], pk[1], pk[2], pk[3]}, rP, pb + 16384, 0, 16);
  wait_lgkm0();
  mfmav<true>(acc[3][0], Wf[0][3], F[0][0]);
  mfmav<true>(acc[3][1], Wf[0][3], F[0][1]);
  mfmav<true>(acc[3][2], Wf[0][3], F[0][2]);
  z[0] = vmul(acc[2][0][0], rl[0]);
  mfmav<true>(acc[3][3], Wf[0][3], F[0][3]);
  mfmav<true>(acc[3][4], Wf[0][3], F[0][4]);
  z[1] = vmul(acc[2][0][1], rl[0]);
  mfmav<false>(acc[3][0], Wf[1][3], F[1][0]);
  z[2] = vmul(acc[2][0][2], rl[0]);
  mfmav<false>(acc[3][1], Wf[1][3], F[1][1]);
  z[3] = vmul(acc[2][0][3], rl[0]);
  mfmav<false>(acc[3][2], Wf[1][3], F[1][2]);
  mfmav<false>(acc[3][3], Wf[1][3], F[1][3]);
  z[4] = vmul(acc[2][1][0], rl[1]);
  mfmav<false>(acc[3][4], Wf[1][3], F[1][4]);
  z[5] = vmul(acc[2][1][1], rl[1]);
  mfmav<false>(acc[3][0], Wf[2][3], F[2][0]);
  mfmav<false>(acc[3][1], Wf[2][3], F[2][1]);
  z[6] = vmul(acc[2][1][2], rl[1]);
  mfmav<false>(acc[3][2], Wf[2][3], F[2][2]);
  z[7] = vmul(acc[2][1][3], rl[1]);
  mfmav<false>(acc[3][3], Wf[2][3], F[2][3]);
  pk[0] = vcvt(z[0], z[1]);
  mfmav<false>(acc[3][4], Wf[2][3], F[2][4]);
  mfmav<false>(acc[3][0], Wf[3][3], F[3][0]);
  pk[1] = vcvt(z[2], z[3]);
  mfmav<false>(acc[3][1], Wf[3][3], F[3][1]);
  pk[2] = vcvt(z[4], z[5]);
  mfmav<false>(acc[3][2], Wf[3][3], F[3][2]);
  pk[3] = vcvt(z[6], z[7]);
  mfmav<false>(acc[3][3], Wf[3][3], F[3][3]);
  mfmav<false>(acc[3][4], Wf[3][3], F[3][4]);
  __builtin_amdgcn_raw_buffer_store_b128(u32x4{pk[0], pk[1], pk[2], pk[3]}, rP, pb + 20480, 0, 16);
  wait_vm<6>();
  barrier();
  mfmav<false>(acc[3][0], Wf[4][3], F[4][0]);
  z[0] = vmul(acc[2][2][0], rl[2]);
  mfmav<false>(acc[3][1], Wf[4][3], F[4][1]);
  A[0][0] = ldtr(lds, aO[0][0] + 0, aO[0][0] + 2048);
  mfmav<false>(acc[3][2], Wf[4][3], F[4][2]);
  A[0][1] = ldtr(lds, aO[0][1] + 0, aO[0][1] + 2048);
  mfmav<false>(acc[3][3], Wf[4][3], F[4][3]);
  A[0][2] = ldtr(lds, aO[0][0] + 30720, aO[0][0] + 32768);
  mfmav<false>(acc[3][4], Wf[4][3], F[4][4]);
  A[0][3] = ldtr(lds, aO[0][1] + 30720, aO[0][1] + 32768);
  mfmav<false>(acc[3][0], Wf[5][3], F[5][0]);
  mfmav<false>(acc[3][1], Wf[5][3], F[5][1]);
  mfmav<false>(acc[3][2], Wf[5][3], F[5][2]);
  mfmav<false>(acc[3][3], Wf[5][3], F[5][3]);
  mfmav<false>(acc[3][4], Wf[5][3], F[5][4]);
  z[1] = vmul(acc[2][2][1], rl[2]);
  z[2] = vmul(acc[2][2][2], rl[2]);
  mfmav<false>(acc[3][0], Wf[6][3], F[6][0]);
  z[3] = vmul(acc[2][2][3], rl[2]);
  mfmav<false>(acc[3][1], Wf[6][3], F[6][1]);
  z[4] = vmul(acc[2][3][0], rl[3]);
  mfmav<false>(acc[3][2], Wf[6][3], F[6][2]);
  z[5] = vmul(acc[2][3][1], rl[3]);
  mfmav<false>(acc[3][3], Wf[6][3], F[6][3]);
  z[6] = vmul(acc[2][3][2], rl[3]);
  mfmav<false>(acc[3][4], Wf[6][3], F[6][4]);
  z[7] = vmul(acc[2][3][3], rl[3]);
  mfmav<false>(acc[3][0], Wf[7][3], F[7][0]);
  pk[0] = vcvt(z[0], z[1]);
  mfmav<false>(acc[3][1], Wf[7][3], F[7][1]);
  pk[1] = vcvt(z[2], z[3]);
  mfmav<false>(acc[3][2], Wf[7][3], F[7][2]);
  pk[2] = vcvt(z[4], z[5]);
  mfmav<false>(acc[3][3], Wf[7][3], F[7][3]);
  pk[3] = vcvt(z[6], z[7]);
  mfmav<false>(acc[3][4], Wf[7][3], F[7][4]);
  __builtin_amdgcn_raw_buffer_store_b128(u32x4{pk[0], pk[1], pk[2], pk[3]}, rP, pb + 24576, 0, 16);
  wait_lgkm0();
  mfmav<true>(acc[4][0], A[0][0], F[0][0]);
  mfmav<true>(acc[4][1], A[0][0], F[0][1]);
  A[1][0] = ldtr(lds, aO[0][0] + 61440, aO[0][0] + 63488);
  mfmav<true>(acc[4][2], A[0][0], F[0][2]);
  A[1][1] = ldtr(lds, aO[0][1] + 61440, aO[0][1] + 63488);
  mfmav<true>(acc[4][3], A[0][0], F[0][3]);
  A[1][2] = ldtr(lds, aO[0][0] + 92160, aO[0][0] + 94208);
  mfmav<true>(acc[4][4], A[0][0], F[0][4]);
  A[1][3] = ldtr(lds, aO[0][1] + 92160, aO[0][1] + 94208);
  mfmav<false>(acc[4][0], A[0][1], F[1][0]);
  mfmav<false>(acc[4][1], A[0][1], F[1][1]);
  z[0] = vmul(acc[2][4][0], rl[4]);
  mfmav<false>(acc[4][2], A[0][1], F[1][2]);
  z[1] = vmul(acc[2][4][1], rl[4]);
  z[2] = vmul(acc[2][4][2], rl[4]);
  mfmav<false>(acc[4][3], A[0][1], F[1][3]);
  z[3] = vmul(acc[2][4][3], rl[4]);
  mfmav<false>(acc[4][4], A[0][1], F[1][4]);
  z[4] = vmul(acc[3][0][0], rl[0]);
  z[5] = vmul(acc[3][0][1], rl[0]);
  mfmav<false>(acc[4][0], A[0][2], F[2][0]);
  z[6] = vmul(acc[3][0][2], rl[0]);
  mfmav<false>(acc[4][1], A[0][2], F[2][1]);
  z[7] = vmul(acc[3][0][3], rl[0]);
  mfmav<false>(acc[4][2], A[0][2], F[2][2]);
  pk[0] = vcvt(z[0], z[1]);
  pk[1] = vcvt(z[2], z[3]);
  mfmav<false>(acc[4][3], A[0][2], F[2][3]);
  pk[2] = vcvt(z[4], z[5]);
  mfmav<false>(acc[4][4], A[0][2], F[2][4]);
  pk[3] = vcvt(z[6], z[7]);
  __builtin_amdgcn_raw_buffer_store_b128(u32x4{pk[0], pk[1], pk[2], pk[3]}, rP, pb + 28672, 0, 16);
  mfmav<false>(acc[4][0], A[0][3], F[3][0]);
  z[0] = vmul(acc[3][1][0], rl[1]);
  mfmav<false>(acc[4][1], A[0][3], F[3][1]);
  z[1] = vmul(acc[3][1][1], rl[1]);
  z[2] = vmul(acc[3][1][2], rl[1]);
  mfmav<false>(acc[4][2], A[0][3], F[3][2]);
  z[3] = vmul(acc[3][1][3], rl[1]);
  mfmav<false>(acc[4][3], A[0][3], F[3][3]);
  z[4] = vmul(acc[3][2][0], rl[2]);
  z[5] = vmul(acc[3][2][1], rl[2]);
  mfmav<false>(acc[4][4], A[0][3], F[3][4]);
  z[6] = vmul(acc[3][2][2], rl[2]);
  wait_lgkm0();
  mfmav<false>(acc[4][0], A[1][0], F[4][0]);
  z[7] = vmul(acc[3][2][3], rl[2]);
  mfmav<false>(acc[4][1], A[1][0], F[4][1]);
  A[0][0] = ldtr(lds, aO[1][0] + 0, aO[1][0] + 2048);
  mfmav<false>(acc[4][2], A[1][0], F[4][2]);
  A[0][1] = ldtr(lds, aO[1][1] + 0, aO[1][1] + 2048);
  mfmav<false>(acc[4][3], A[1][0], F[4][3]);
  A[0][2] = ldtr(lds, aO[1][0] + 30720, aO[1][0] + 32768);
  mfmav<false>(acc[4][4], A[1][0], F[4][4]);
  A[0][3] = ldtr(lds, aO[1][1] + 30720, aO[1][1] + 32768);
  mfmav<false>(acc[4][0], A[1][1], F[5][0]);
  mfmav<false>(acc[4][1], A[1][1], F[5][1]);
  pk[0] = vcvt(z[0], z[1]);
  mfmav<false>(acc[4][2], A[1][1], F[5][2]);
  pk[1] = vcvt(z[2], z[3]);
  pk[2] = vcvt(z[4], z[5]);
  mfmav<false>(acc[4][3], A[1][1], F[5][3]);
  pk[3] = vcvt(z[6], z[7]);
  mfmav<false>(acc[4][4], A[1][1], F[5][4]);
  __builtin_amdgcn_raw_buffer_store_b128(u32x4{pk[0], pk[1], pk[2], pk[3]}, rP, pb + 32768, 0, 16);
  mfmav<false>(acc[4][0], A[1][2], F[6][0]);
  z[0] = vmul(acc[3][3][0], rl[3]);
  z[1] = vmul(acc[3][3][1], rl[3]);
  mfmav<false>(acc[4][1], A[1][2], F[6][1]);
  z[2] = vmul(acc[3][3][2], rl[3]);
  mfmav<false>(acc[4][2], A[1][2], F[6][2]);
  z[3] = vmul(acc[3][3][3], rl[3]);
  z[4] = vmul(acc[3][4][0], rl[4]);
  mfmav<false>(acc[4][3], A[1][2], F[6][3]);
  z[5] = vmul(acc[3][4][1], rl[4]);
  mfmav<false>(acc[4][4], A[1][2], F[6][4]);
  z[6] = vmul(acc[3][4][2], rl[4]);
  z[7] = vmul(acc[3][4][3], rl[4]);
  mfmav<false>(acc[4][0], A[1][3], F[7][0]);
  pk[0] = vcvt(z[0], z[1]);
  mfmav<false>(acc[4][1], A[1][3], F[7][1]);
  pk[1] = vcvt(z[2], z[3]);
  pk[2] = vcvt(z[4], z[5]);
  mfmav<false>(acc[4][2], A[1][3], F[7][2]);
  pk[3] = vcvt(z[6], z[7]);
  mfmav<false>(acc[4][3], A[1][3], F[7][3]);
  __builtin_amdgcn_raw_buffer_store_b128(u32x4{pk[0], pk[1], pk[2], pk[3]}, rP, pb + 36864, 0, 16);
  mfmav<false>(acc[4][4], A[1][3], F[7][4]);
  wait_vm<5>();
  PUBLISH(0, 5);
  wait_lgkm0();
  mfmav<true>(acc[5][0], A[0][0], F[0][0]);
  mfmav<true>(acc[5][1], A[0][0], F[0][1]);
  A[1][0] = ldtr(lds, aO[1][0] + 61440, aO[1][0] + 63488);
  mfmav<true>(acc[5][2], A[0][0], F[0][2]);
  A[1][1] = ldtr(lds, aO[1][1] + 61440, aO[1][1] + 63488);
  mfmav<true>(acc[5][3], A[0][0], F[0][3]);
  A[1][2] = ldtr(lds, aO[1][0] + 92160, aO[1][0] + 94208);
  mfmav<true>(acc[5][4], A[0][0], F[0][4]);
  A[1][3] = ldtr(lds, aO[1][1] + 92160, aO[1][1] + 94208);
  mfmav<false>(acc[5][0], A[0][1], F[1][0]);
  mfmav<false>(acc[5][1], A[0][1], F[1][1]);
  mfmav<false>(acc[5][2], A[0][1], F[1][2]);
  mfmav<false>(acc[5][3], A[0][1], F[1][3]);
  z[0] = vmul(acc[4][0][0], rl[0]);
  mfmav<false>(acc[5][4], A[0][1], F[1][4]);
  z[1] = vmul(acc[4][0][1], rl[0]);
  mfmav<false>(acc[5][0], A[0][2], F[2][0]);
  z[2] = vmul(acc[4][0][2], rl[0]);
  z[3] = vmul(acc[4][0][3], rl[0]);
  mfmav<false>(acc[5][1], A[0][2], F[2][1]);
  z[4] = vmul(acc[4][1][0], rl[1]);
  mfmav<false>(acc[5][2], A[0][2], F[2][2]);
  z[5] = vmul(acc[4][1][1], rl[1]);
  mfmav<false>(acc[5][3], A[0][2], F[2][3]);
  z[6] = vmul(acc[4][1][2], rl[1]);
  mfmav<false>(acc[5][4], A[0][2], F[2][4]);
  z[7] = vmul(acc[4][1][3], rl[1]);
  mfmav<false>(acc[5][0], A[0][3], F[3][0]);
  pk[0] = vcvt(z[0], z[1]);
  mfmav<false>(acc[5][1], A[0][3], F[3][1]);
  pk[1] = vcvt(z[2], z[3]);
  mfmav<false>(acc[5][2], A[0][3], F[3][2]);
  pk[2] = vcvt(z[4], z[5]);
  mfmav<false>(acc[5][3], A[0][3], F[3][3]);
  pk[3] = vcvt(z[6], z[7]);
  mfmav<false>(acc[5][4], A[0][3], F[3][4]);
  __builtin_amdgcn_raw_buffer_store_b128(u32x4{pk[0], pk[1], pk[2], pk[3]}, rP, pb + 40960, 0, 16);
  wait_lgkm0();
  mfmav<false>(acc[5][0], A[1][0], F[4][0]);
  z[0] = vmul(acc[4][2][0], rl[2]);
  mfmav<false>(acc[5][1], A[1][0], F[4][1]);
  A[0][0] = ldtr(lds, aO[2][0] + 0, aO[2][0] + 2048);
  mfmav<false>(acc[5][2], A[1][0], F[4][2]);
  A[0][1] = ldtr(lds, aO[2][1] + 0, aO[2][1] + 2048);
  mfmav<false>(acc[5][3], A[1][0], F[4][3]);
  A[0][2] = ldtr(lds, aO[2][0] + 30720, aO[2][0] + 32768);
  mfmav<false>(acc[5][4], A[1][0], F[4][4]);
  A[0][3] = ldtr(lds, aO[2][1] + 30720, aO[2][1] + 32768);
  mfmav<false>(acc[5][0], A[1][1], F[5][0]);
  mfmav<false>(acc[5][1], A[1][1], F[5][1]);
  mfmav<false>(acc[5][2], A[1][1], F[5][2]);
  mfmav<false>(acc[5][3], A[1][1], F[5][3]);
  mfmav<false>(acc[5][4], A[1][1], F[5][4]);
  z[1] = vmul(acc[4][2][1], rl[2]);
  z[2] = vmul(acc[4][2][2], rl[2]);
  mfmav<false>(acc[5][0], A[1][2], F[6][0]);
  z[3] = vmul(acc[4][2][3], rl[2]);
  mfmav<false>(acc[5][1], A[1][2], F[6][1]);
  z[4] = vmul(acc[4][3][0], rl[3]);
  mfmav<false>(acc[5][2], A[1][2], F[6][2]);
  z[5] = vmul(acc[4][3][1], rl[3]);
  mfmav<false>(acc[5][3], A[1][2], F[6][3]);
  z[6] = vmul(acc[4][3][2], rl[3]);
  mfmav<false>(acc[5][4], A[1][2], F[6][4]);
  z[7] = vmul(acc[4][3][3], rl[3]);
  mfmav<false>(acc[5][0], A[1][3], F[7][0]);
  pk[0] = vcvt(z[0], z[1]);
  mfmav<false>(acc[5][1], A[1][3], F[7][1]);
  pk[1] = vcvt(z[2], z[3]);
  mfmav<false>(acc[5][2], A[1][3], F[7][2]);
  pk[2] = vcvt(z[4], z[5]);
  mfmav<false>(acc[5][3], A[1][3], F[7][3]);
  pk[3] = vcvt(z[6], z[7]);
  mfmav<false>(acc[5][4], A[1][3], F[7][4]);
  __builtin_amdgcn_raw_buffer_store_b128(u32x4{pk[0], pk[1], pk[2], pk[3]}, rP, pb + 45056, 0, 16);
  wait_lgkm0();
  mfmav<true>(acc[6][0], A[0][0], F[0][0]);
  mfmav<true>(acc[6][1], A[0][0], F[0][1]);
  A[1][0] = ldtr(lds, aO[2][0] + 61440, aO[2][0] + 63488);
  mfmav<true>(acc[6][2], A[0][0], F[0][2]);
  A[1][1] = ldtr(lds, aO[2][1] + 61440, aO[2][1] + 63488);
  mfmav<true>(acc[6][3], A[0][0], F[0][3]);
  A[1][2] = ldtr(lds, aO[2][0] + 92160, aO[2][0] + 94208);
  mfmav<true>(acc[6][4], A[0][0], F[0][4]);
  A[1][3] = ldtr(lds, aO[2][1] + 92160, aO[2][1] + 94208);
  mfmav<false>(acc[6][0], A[0][1], F[1][0]);
  mfmav<false>(acc[6][1], A[0][1], F[1][1]);
  z[0] = vmul(acc[4][4][0], rl[4]);
  mfmav<false>(acc[6][2], A[0][1], F[1][2]);
  z[1] = vmul(acc[4][4][1], rl[4]);
  z[2] = vmul(acc[4][4][2], rl[4]);
  mfmav<false>(acc[6][3], A[0][1], F[1][3]);
  z[3] = vmul(acc[4][4][3], rl[4]);
  mfmav<false>(acc[6][4], A[0][1], F[1][4]);
  z[4] = vmul(acc[5][0][0], rl[0]);
  z[5] = vmul(acc[5][0][1], rl[0]);
  mfmav<false>(acc[6][0], A[0][2], F[2][0]);
  z[6] = vmul(acc[5][0][2], rl[0]);
  mfmav<false>(acc[6][1], A[0][2], F[2][1]);
  z[7] = vmul(acc[5][0][3], rl[0]);
  mfmav<false>(acc[6][2], A[0][2], F[2][2]);
  pk[0] = vcvt(z[0], z[1]);
  pk[1] = vcvt(z[2], z[3]);
  mfmav<false>(acc[6][3], A[0][2], F[2][3]);
  pk[2] = vcvt(z[4], z[5]);
  mfmav<false>(acc[6][4], A[0][2], F[2][4]);
  pk[3] = vcvt(z[6], z[7]);
  __builtin_amdgcn_raw_buffer_store_b128(u32x4{pk[0], pk[1], pk[2], pk[3]}, rP, pb + 49152, 0, 16);
  mfmav<false>(acc[6][0], A[0][3], F[3][0]);
  z[0] = vmul(acc[5][1][0], rl[1]);
  mfmav<false>(acc[6][1], A[0][3], F[3][1]);
  z[1] = vmul(acc[5][1][1], rl[1]);
  z[2] = vmul(acc[5][1][2], rl[1]);
  mfmav<false>(acc[6][2], A[0][3], F[3][2]);
  z[3] = vmul(acc[5][1][3], rl[1]);
  mfmav<false>(acc[6][3], A[0][3], F[3][3]);
  z[4] = vmul(acc[5][2][0], rl[2]);
  z[5] = vmul(acc[5][2][1], rl[2]);
  mfmav<false>(acc[6][4], A[0][3], F[3][4]);
  z[6] = vmul(acc[5][2][2], rl[2]);
  wait_lgkm0();
  mfmav<false>(acc[6][0], A[1][0], F[4][0]);
  z[7] = vmul(acc[5][2][3], rl[2]);
  pk[0] = vcvt(z[0], z[1]);
  mfmav<false>(acc[6][0], A[1][1], F[5][0]);
  pk[1] = vcvt(z[2], z[3]);
  mfmav<false>(acc[6][0], A[1][2], F[6][0]);
  pk[2] = vcvt(z[4], z[5]);
  pk[3] = vcvt(z[6], z[7]);
  mfmav<false>(acc[6][0], A[1][3], F[7][0]);
  __builtin_amdgcn_raw_buffer_store_b128(u32x4{pk[0], pk[1], pk[2], pk[3]}, rP, pb + 53248, 0, 16);
  z[0] = vmul(acc[5][3][0], rl[3]);
  mfmav<false>(acc[6][1], A[1][0], F[4][1]);
  z[1] = vmul(acc[5][3][1], rl[3]);
  mfmav<false>(acc[6][1], A[1][1], F[5][1]);
  z[2] = vmul(acc[5][3][2], rl[3]);
  z[3] = vmul(acc[5][3][3], rl[3]);
  mfmav<false>(acc[6][1], A[1][2], F[6][1]);
  z[4] = vmul(acc[5][4][0], rl[4]);
  z[5] = vmul(acc[5][4][1], rl[4]);
  mfmav<false>(acc[6][1], A[1][3], F[7][1]);
  z[6] = vmul(acc[5][4][2], rl[4]);
  z[7] = vmul(acc[5][4][3], rl[4]);
  mfmav<false>(acc[6][2], A[1][0], F[4][2]);
  pk[0] = vcvt(z[0], z[1]);
  mfmav<false>(acc[6][2], A[1][1], F[5][2]);
  pk[1] = vcvt(z[2], z[3]);
  pk[2] = vcvt(z[4], z[5]);
  mfmav<false>(acc[6][2], A[1][2], F[6][2]);
  pk[3] = vcvt(z[6], z[7]);
  __builtin_amdgcn_raw_buffer_store_b128(u32x4{pk[0], pk[1], pk[2], pk[3]}, rP, pb + 57344, 0, 16);
  mfmav<false>(acc[6][2], A[1][3], F[7][2]);
  wait_vm<6>();
  PUBLISH(5, 5);
  mfmav<false>(acc[6][3], A[1][0], F[4][3]);
  mfmav<false>(acc[6][3], A[1][1], F[5][3]);
  z[0] = vmul(acc[6][0][0], rl[0]);
  z[1] = vmul(acc[6][0][1], rl[0]);
  mfmav<false>(acc[6][3], A[1][2], F[6][3]);
  z[2] = vmul(acc[6][0][2], rl[0]);
  z[3] = vmul(acc[6][0][3], rl[0]);
  mfmav<false>(acc[6][3], A[1][3], F[7][3]);
  z[4] = vmul(acc[6][1][0], rl[1]);
  z[5] = vmul(acc[6][1][1], rl[1]);
  mfmav<false>(acc[6][4], A[1][0], F[4][4]);
  z[6] = vmul(acc[6][1][2], rl[1]);
  z[7] = vmul(acc[6][1][3], rl[1]);
  mfmav<false>(acc[6][4], A[1][1], F[5][4]);
  pk[0] = vcvt(z[0], z[1]);
  pk[1] = vcvt(z[2], z[3]);
  mfmav<false>(acc[6][4], A[1][2], F[6][4]);
  pk[2] = vcvt(z[4], z[5]);
  pk[3] = vcvt(z[6], z[7]);
  mfmav<false>(acc[6][4], A[1][3], F[7][4]);
  __builtin_amdgcn_raw_buffer_store_b128(u32x4{pk[0], pk[1], pk[2], pk[3]}, rP, pb + 61440, 0, 16);
  mfma_pad();
  z[0] = vmul(acc[6][2][0], rl[2]);
  z[1] = vmul(acc[6][2][1], rl[2]);
  z[2] = vmul(acc[6][2][2], rl[2]);
  z[3] = vmul(acc[6][2][3], rl[2]);
  z[4] = vmul(acc[6][3][0], rl[3]);
  z[5] = vmul(acc[6][3][1], rl[3]);
  z[6] = vmul(acc[6][3][2], rl[3]);
  z[7] = vmul(acc[6][3][3], rl[3]);
  pk[0] = vcvt(z[0], z[1]);
  pk[1] = vcvt(z[2], z[3]);
  pk[2] = vcvt(z[4], z[5]);
  pk[3] = vcvt(z[6], z[7]);
  __builtin_amdgcn_raw_buffer_store_b128(u32x4{pk[0], pk[1], pk[2], pk[3]}, rP, pb + 65536, 0, 16);
  z[0] = vmul(acc[6][4][0], rl[4]);
  z[1] = vmul(acc[6][4][1], rl[4]);
  z[2] = vmul(acc[6][4][2], rl[4]);
  z[3] = vmul(acc[6][4][3], rl[4]);
  pk[0] = vcvt(z[0], z[1]);
  pk[1] = vcvt(z[2], z[3]);
  POLL();
  __builtin_amdgcn_raw_buffer_store_b64(u32x2{pk[0], pk[1]}, rP, pbh, 0, 16);
  OUT_ADDR();
  #define K2_ 10
  asm volatile("s_waitcnt vmcnt(0)" : "+v"(fl) :: "memory");
  PUBLISH(K2_, 18 - K2_);
  char* const ob = (char*)out;
  u64 f1 = 0, f2 = 0, f3 = 0;
  if (!__all(fl == token)) { POLL_START(fp0); POLL_WAIT(fp0); }
  if (wave == 0 && h < 6) {
    GAS u64* const fp1 = flags + (h < 4 ? (64 + h) * 16 + (lt & 15) : (60 + 2 * h) * 16 + (lt & 31));
    GATHER_REDUCE(BLK0, 0, POLL_START(fp1), 4);
    if (h == 0 && lt == 0) __hip_atomic_store(epoch, token, __ATOMIC_RELAXED, __HIP_MEMORY_SCOPE_AGENT);
    asm volatile("s_waitcnt vmcnt(2)" : "+v"(fl), "+v"(f1), "+v"(f2), "+v"(f3) :: "memory");
    if (!__all(f3 == token)) { POLL_START(fp1); POLL_WAIT(fp1); }
    GATHER_REDUCE(64 + h, 1, , 0);
  } else {
    GATHER_REDUCE(BLK0, 0, , 0);
  }
  asm volatile("gqa_lbl_z:");
}

extern "C" void kernel_launch(void* const* d_in, const int* in_sizes, int n_in, void* d_out, int out_size, void* d_ws, size_t ws_size, hipStream_t stream) {
  (void)hipFuncSetAttribute((const void*)gqa_fused, hipFuncAttributeMaxDynamicSharedMemorySize, LDS_BYTES);
  gqa_fused<<<256, 256, LDS_BYTES, stream>>>((const u16*)d_in[0], (const u16*)d_in[1], (const u16*)d_in[2], (const u16*)d_in[3], (const u16*)d_in[4], (u16*)d_out, (char*)d_ws);
}
```
